```python
import torch

def _softmax(x, axis=-1):
    return torch.softmax(x, dim=axis)
def _gelu(x):
    return torch.nn.functional.gelu(x, approximate="tanh")
def _silu(x):
    return torch.nn.functional.silu(x)
def _sigmoid(x):
    return torch.sigmoid(x)
def _layernorm(x, axis=-1, eps=1e-5):
    m = x.mean(dim=axis, keepdim=True)
    v = x.var(dim=axis, keepdim=True, unbiased=False)
    return (x - m) / torch.sqrt(v + eps)
def _scatter_add(tgt, idx, src, axis=0):
    return tgt.clone().index_add_(axis, idx, src)
def _segment_sum(src, seg, n):
    out = torch.zeros((n,) + src.shape[1:], dtype=src.dtype)
    return out.index_add_(0, seg, src)
def _pad(x, pw):
    flat = tuple(v for pair in reversed(pw) for v in pair)
    return torch.nn.functional.pad(x, flat)

kernel_name = 'recurrent_scan__grouped_kv_mqa__swiglu_ffn__prompt'

def setup_inputs():

    _w = (lambda a: (lambda t: t.to(torch.bfloat16) if t.is_floating_point() else t)(torch.as_tensor(a)))
    def _A_setup():
        B, S, D, H = 1, 2048, 640, 32
        Dh = D // H
        return {
            "x": _w((lambda *s: torch.randn(*s, dtype=torch.bfloat16) * 0.05)(B, S, D)),
            "wq": _w((lambda *s: torch.randn(*s, dtype=torch.bfloat16) * 0.05)(D, H * Dh)),
            "wk": _w((lambda *s: torch.randn(*s, dtype=torch.bfloat16) * 0.05)(D, Dh)),
            "wv": _w((lambda *s: torch.randn(*s, dtype=torch.bfloat16) * 0.05)(D, Dh)),
            "wo": _w((lambda *s: torch.randn(*s, dtype=torch.bfloat16) * 0.05)(H * Dh, D)),
        }
    _A_in = _A_setup()

    def _B_setup():
        B, S, D, F = 1, 2048, 640, 1728
        return {
            "x": _w((lambda *s: torch.randn(*s, dtype=torch.bfloat16) * 0.05)(B, S, D)),
            "w_gate": _w((lambda *s: torch.randn(*s, dtype=torch.bfloat16) * 0.05)(D, F)),
            "w_up": _w((lambda *s: torch.randn(*s, dtype=torch.bfloat16) * 0.05)(D, F)),
            "w_down": _w((lambda *s: torch.randn(*s, dtype=torch.bfloat16) * 0.05)(F, D)),
        }
    _B_in = _B_setup()

    return {
        "A_x": _A_in["x"],
        "A_wq": _A_in["wq"],
        "A_wk": _A_in["wk"],
        "A_wv": _A_in["wv"],
        "A_wo": _A_in["wo"],
        "B_x": _B_in["x"],
        "B_w_gate": _B_in["w_gate"],
        "B_w_up": _B_in["w_up"],
        "B_w_down": _B_in["w_down"],
    }

def reference(A_x, A_wq, A_wk, A_wv, A_wo, B_x, B_w_gate, B_w_up, B_w_down):
    def _A_ref(x, wq, wk, wv, wo):
        B, S, D = x.shape
        H = 32
        Dh = D // H
        q = (lambda x, *d: x.permute(*d))((x @ wq).reshape(B, S, H, Dh), 0, 2, 1, 3)
        k = x @ wk
        v = x @ wv
        scores = torch.einsum("bhqd,bkd->bhqk", q, k) * (Dh ** -0.5)
        p = _softmax(scores, axis=-1)
        o = torch.einsum("bhqk,bkd->bhqd", p, v)
        return (lambda x, *d: x.permute(*d))(o, 0, 2, 1, 3).reshape(B, S, H * Dh) @ wo

    def _B_ref(x, w_gate, w_up, w_down):
        g = _silu(x @ w_gate)
        u = x @ w_up
        return (g * u) @ w_down

    x = A_x
    xn = _layernorm(x, axis=-1)
    s = _A_ref(xn, A_wq, A_wk, A_wv, A_wo)
    g = _silu(_B_ref(xn, B_w_gate, B_w_up, B_w_down))
    return x + s * g
```

```cpp
#include <hip/hip_runtime.h>
#include <hip/hip_bf16.h>
typedef unsigned short u16;
__device__ __forceinline__ float bf2f(u16 v){ return __uint_as_float(((unsigned)v)<<16); }
__device__ __forceinline__ u16 f2bf(float f){ unsigned u=__float_as_uint(f); u += 0x7FFF + ((u>>16)&1); return (u16)(u>>16); }
constexpr int S=2048,D=640,H=32,DH=20,F=1728;
__global__ void ln_k(const u16* x, float* xn){
  int r=blockIdx.x; __shared__ float red[2][256]; float s=0,ss=0;
  for(int i=threadIdx.x;i<D;i+=256){ float v=bf2f(x[r*D+i]); s+=v; }
  red[0][threadIdx.x]=s; __syncthreads();
  if(threadIdx.x==0){ float t=0; for(int i=0;i<256;i++) t+=red[0][i]; red[1][0]=t/D; } __syncthreads();
  float m=red[1][0]; __syncthreads();
  for(int i=threadIdx.x;i<D;i+=256){ float v=bf2f(x[r*D+i])-m; ss+=v*v; }
  red[0][threadIdx.x]=ss; __syncthreads();
  if(threadIdx.x==0){ float t=0; for(int i=0;i<256;i++) t+=red[0][i]; red[1][1]=rsqrtf(t/D+1e-5f); } __syncthreads();
  float rs=red[1][1];
  for(int i=threadIdx.x;i<D;i+=256){ xn[r*D+i]=(bf2f(x[r*D+i])-m)*rs; }
}
__global__ void gemm_k(const float* A, const u16* W, float* C, int N, int K){
  int n=blockIdx.x*256+threadIdx.x; int m=blockIdx.y; if(n>=N) return; float acc=0;
  for(int k=0;k<K;k++) acc+=A[(long)m*K+k]*bf2f(W[(long)k*N+n]);
  C[(long)m*N+n]=acc;
}
__global__ void attn_k(const float* q,const float* k,const float* v,float* o){
  int idx=blockIdx.x*256+threadIdx.x; int t=idx/H, h=idx%H; float qq[DH];
  #pragma unroll
  for(int d=0;d<DH;d++) qq[d]=q[t*D+h*DH+d]*0.22360679775f;
  float m=-1e30f,l=0; float acc[DH];
  #pragma unroll
  for(int d=0;d<DH;d++) acc[d]=0;
  for(int j=0;j<S;j++){ float s=0;
    #pragma unroll
    for(int d=0;d<DH;d++) s+=qq[d]*k[j*DH+d];
    float mn=fmaxf(m,s); float a=__expf(m-mn), p=__expf(s-mn); l=l*a+p;
    #pragma unroll
    for(int d=0;d<DH;d++) acc[d]=acc[d]*a+p*v[j*DH+d];
    m=mn; }
  #pragma unroll
  for(int d=0;d<DH;d++) o[t*D+h*DH+d]=acc[d]/l;
}
__global__ void glu_k(const float* g,const float* u,float* h,int n){ int i=blockIdx.x*256+threadIdx.x; if(i<n){ float x=g[i]; h[i]=x/(1.f+__expf(-x))*u[i]; } }
__global__ void fin_k(const u16* x,const float* s,const float* d,u16* out,int n){ int i=blockIdx.x*256+threadIdx.x; if(i<n){ float z=d[i]; out[i]=f2bf(bf2f(x[i])+s[i]*(z/(1.f+__expf(-z)))); } }
extern "C" void kernel_launch(void* const* d_in, const int* in_sizes, int n_in, void* d_out, int out_size, void* d_ws, size_t ws_size, hipStream_t stream){
  const u16* x=(const u16*)d_in[0]; const u16* wq=(const u16*)d_in[1]; const u16* wk=(const u16*)d_in[2]; const u16* wv=(const u16*)d_in[3]; const u16* wo=(const u16*)d_in[4];
  const u16* wg=(const u16*)d_in[6]; const u16* wu=(const u16*)d_in[7]; const u16* wd=(const u16*)d_in[8];
  float* w=(float*)d_ws; float* xn=w; float* q=xn+S*D; float* k=q+S*D; float* v=k+S*DH; float* o=v+S*DH; float* s=o+S*D; float* g=s+S*D; float* u=g+S*F; float* h=u+S*F; float* dd=h+S*F;
  ln_k<<<S,256,0,stream>>>(x,xn);
  gemm_k<<<dim3(3,S),256,0,stream>>>(xn,wq,q,D,D);
  gemm_k<<<dim3(1,S),256,0,stream>>>(xn,wk,k,DH,D);
  gemm_k<<<dim3(1,S),256,0,stream>>>(xn,wv,v,DH,D);
  attn_k<<<S*H/256,256,0,stream>>>(q,k,v,o);
  gemm_k<<<dim3(3,S),256,0,stream>>>(o,wo,s,D,D);
  gemm_k<<<dim3(7,S),256,0,stream>>>(xn,wg,g,F,D);
  gemm_k<<<dim3(7,S),256,0,stream>>>(xn,wu,u,F,D);
  glu_k<<<(S*F+255)/256,256,0,stream>>>(g,u,h,S*F);
  gemm_k<<<dim3(3,S),256,0,stream>>>(h,wd,dd,D,F);
  fin_k<<<(S*D+255)/256,256,0,stream>>>(x,s,dd,(u16*)d_out,S*D);
}
```

```cpp
#include <hip/hip_runtime.h>
#include <hip/hip_bf16.h>

typedef unsigned short u16;
typedef __bf16 bf16x2 __attribute__((ext_vector_type(2)));
typedef __bf16 bf16x8 __attribute__((ext_vector_type(8)));
typedef __bf16 bf16x16 __attribute__((ext_vector_type(16)));
typedef short s16x4 __attribute__((ext_vector_type(4)));
typedef float f32x16 __attribute__((ext_vector_type(16)));
typedef float f32x8 __attribute__((ext_vector_type(8)));
typedef float f32x4 __attribute__((ext_vector_type(4)));
typedef float f32x2 __attribute__((ext_vector_type(2)));
typedef unsigned u32x4 __attribute__((ext_vector_type(4)));
typedef unsigned u32x2 __attribute__((ext_vector_type(2)));
typedef __amdgpu_buffer_rsrc_t rsrc_t;
#define LAS __attribute__((address_space(3)))
typedef LAS char* lds_ptr;

#define SBAR() __builtin_amdgcn_sched_barrier(0)
#define PIN(x) asm volatile("" : "+v"(x))
#define DEVI __device__ __forceinline__

constexpr int S_ = 2048, D_ = 640, F_ = 1728;
constexpr int REC = 2624;
constexpr size_t WS_QG = 0;
constexpr size_t WS_KV = WS_QG + 64 * 160 * 256;
constexpr size_t WS_HF = WS_KV + 64 * REC + 4096 - (64 * REC) % 4096;
constexpr size_t WS_OF = WS_HF + 128 * 54 * 1024;
constexpr size_t WS_DN = WS_OF + 128 * 20 * 1024;
constexpr size_t WS_END = WS_DN + 256 * 20 * 1024;

#define STAMP(k,i)
#define STAMP_NOVM(k,i)
#define STAMP_FLUSH(k,i)
DEVI rsrc_t mkrsrc(const void* p, unsigned bytes) { return __builtin_amdgcn_make_buffer_rsrc((void*)p, 0, bytes, 0x00020000); }
DEVI void dma16(unsigned lds, unsigned voff, rsrc_t rs, unsigned soff) {
    asm volatile("s_mov_b32 m0, %0\n\ts_nop 0\n\tbuffer_load_dwordx4 %1, %2, %3 offen lds" ::"s"(lds), "v"(voff), "s"(rs), "s"(soff) : "memory");
}
DEVI void dma16_lo20(unsigned lds, unsigned voff, rsrc_t rs, unsigned soff) {
    asm volatile("s_mov_b32 m0, %0\n\ts_mov_b64 exec, 0xfffff\n\tbuffer_load_dwordx4 %1, %2, %3 offen lds\n\ts_mov_b64 exec, -1" ::"s"(lds), "v"(voff), "s"(rs), "s"(soff) : "memory");
}
DEVI u32x4 ldsr128(lds_ptr p) { return *(LAS u32x4*)p; }
DEVI u32x2 ldstr(lds_ptr p) { return __builtin_bit_cast(u32x2, __builtin_amdgcn_ds_read_tr16_b64_v4i16((LAS s16x4*)p)); }
DEVI u32x4 ldstr2(lds_ptr p0, lds_ptr p1) { u32x2 a = ldstr(p0), b = ldstr(p1); return (u32x4){a.x, a.y, b.x, b.y}; }
DEVI f32x16 mfma32(u32x4 a, u32x4 b, f32x16 c) { return __builtin_amdgcn_mfma_f32_32x32x16_bf16(__builtin_bit_cast(bf16x8, a), __builtin_bit_cast(bf16x8, b), c, 0, 0, 0); }
DEVI f32x4 mfma16(u32x4 a, u32x4 b, f32x4 c) { return __builtin_amdgcn_mfma_f32_16x16x32_bf16(__builtin_bit_cast(bf16x8, a), __builtin_bit_cast(bf16x8, b), c, 0, 0, 0); }
DEVI float dot2(unsigned a, unsigned b, float c) { return __builtin_amdgcn_fdot2_f32_bf16(__builtin_bit_cast(bf16x2, a), __builtin_bit_cast(bf16x2, b), c, false); }
DEVI unsigned pk2(float lo, float hi) { bf16x2 v = {(__bf16)lo, (__bf16)hi}; return __builtin_bit_cast(unsigned, v); }
DEVI float bfr(float x) { return (float)(__bf16)x; }
DEVI float xhalf(float v) { return __shfl_xor(v, 32); }
constexpr unsigned ONES = 0x3f803f80u;
constexpr float LOG2E = 1.4426950408889634f;

constexpr int G1_X = 0, G1_W = 32768, G1_KW = 49152, G1_SLOT = 51712;
constexpr int G1_CS = 3 * G1_SLOT + 64;
constexpr int G1_ST = G1_CS + 2048;
constexpr int G1_CW = G1_ST + 2048;
constexpr int G1_LDS = G1_CW + 2048;

__global__ __launch_bounds__(512, 2) void k1_proj(const u16* __restrict__ x, const u16* __restrict__ wq, const u16* __restrict__ wk, const u16* __restrict__ wv,
                                                   const u16* __restrict__ wg, const u16* __restrict__ wu, char* __restrict__ ws) {
    extern __shared__ __attribute__((aligned(16))) char smem[];
    const lds_ptr lds = (lds_ptr)smem;
    const unsigned lds0 = (unsigned)(size_t)lds;
    const int lane = threadIdx.x & 63, w = __builtin_amdgcn_readfirstlane(threadIdx.x >> 6);
    const int b = blockIdx.x, xcd = b & 7, bi = b >> 3;
    const int rg = 4 * (xcd & 1) + (bi & 3), cg = 8 * (xcd >> 1) + (bi >> 2);
    const bool isq = cg >= 27;
    const int mi = w & 3, ni = w >> 2;
    const rsrc_t rx = mkrsrc(x, S_ * D_ * 2);
    const rsrc_t rw0 = isq ? mkrsrc(wq, D_ * D_ * 2) : mkrsrc(wg, D_ * F_ * 2);
    const rsrc_t rw1 = isq ? mkrsrc(wq, D_ * D_ * 2) : mkrsrc(wu, D_ * F_ * 2);
    const rsrc_t rkv = mkrsrc((cg & 1) ? wv : wk, D_ * 20 * 2);
    const unsigned pitch = isq ? D_ * 2 : F_ * 2;
    const unsigned wcol0 = isq ? (cg - 27) * 256 : cg * 128, wcol1 = isq ? wcol0 + 128 : wcol0;
    const unsigned vx0 = (lane >> 3) * 1280 + (((lane & 7) ^ (lane >> 4)) << 4);
    const unsigned vx1 = (lane >> 3) * 1280 + (((lane & 7) ^ (4 + (lane >> 4))) << 4);
    const unsigned vw = (lane >> 3) * pitch + (((lane & 7) ^ (((lane >> 4) & 1) << 2)) << 4);
    const unsigned vk = lane * 16;
    const unsigned sx = (256 * rg + 32 * w) * 1280, sw = 8 * w * pitch;
    auto stage = [&](int ks, int slot) {
        const unsigned lb = lds0 + slot * G1_SLOT;
        dma16(lb + G1_X + w * 4096, vx0, rx, sx + ks * 128);
        dma16(lb + G1_X + w * 4096 + 1024, vx1, rx, sx + 8 * 1280 + ks * 128);
        dma16(lb + G1_X + w * 4096 + 2048, vx0, rx, sx + 16 * 1280 + ks * 128);
        dma16(lb + G1_X + w * 4096 + 3072, vx1, rx, sx + 24 * 1280 + ks * 128);
        dma16(lb + G1_W + w * 1024, vw, rw0, sw + ks * 64 * pitch + wcol0);
        dma16(lb + G1_W + 8192 + w * 1024, vw, rw1, sw + ks * 64 * pitch + wcol1);
        dma16_lo20(lb + G1_KW + w * 320, vk, rkv, ks * 2560 + w * 320);
    };
    STAMP(0,0);
    stage(0, 0); stage(1, 1); stage(2, 2);

    const int t = lane & 31, h = lane >> 5;
    int xa[4], wa[4];
    {
        const int xb = (64 * mi + 32 * ni + t) * 128 + ((h ^ ((t >> 1) & 7)) << 4);
        const int blk = (lane >> 4) & 1, q = (lane & 15) >> 2, p = lane & 3;
        const int wb = G1_W + (8 * h + q) * 128 + (((4 * ni + 2 * blk + (p >> 1)) ^ ((q >> 1) << 2)) << 4) + 8 * (p & 1);
#pragma unroll
        for (int s = 0; s < 4; ++s) { const int sa = (s + mi) & 3; xa[s] = xb ^ (sa << 5); wa[s] = wb + sa * 2048; }
    }
    const int kt = lane & 15, kg = lane >> 4;
    const int krow = 16 * (cg >> 1) + kt;
    const int kxa = krow * 128 + ((kg ^ ((krow >> 1) & 7)) << 4);
    const int kwa = G1_KW + (8 * kg + ((lane & 15) >> 2)) * 40 + (16 * w + 4 * (lane & 3)) * 2;

    f32x16 acc[2][2] = {};
    f32x4 kacc = {};
    float rsum = 0.f, rsq = 0.f, cs0 = 0.f, cs1 = 0.f, kcs = 0.f;
    u32x4 xf[2][2], wf[2][2];
    auto ldfr = [&](int set, int slot, int s) {
        const lds_ptr sb = lds + slot * G1_SLOT;
        xf[set][0] = ldsr128(sb + xa[s]);
        xf[set][1] = ldsr128(sb + (xa[s] ^ 4096));
        wf[set][0] = ldstr2(sb + wa[s], sb + wa[s] + 512);
        wf[set][1] = ldstr2(sb + wa[s] + 8192, sb + wa[s] + 8192 + 512);
    };
    auto mm = [&](int set, bool docs) {
        SBAR();
#pragma unroll
        for (int j = 0; j < 4; ++j) {
            acc[j >> 1][j & 1] = mfma32(wf[set][j & 1], xf[set][j >> 1], acc[j >> 1][j & 1]);
            rsum = dot2(xf[set][0][j], ONES, rsum); rsq = dot2(xf[set][0][j], xf[set][0][j], rsq);
            if (docs) { cs0 = dot2(wf[set][0][j], ONES, cs0); cs1 = dot2(wf[set][1][j], ONES, cs1); PIN(cs0); PIN(cs1); }
            PIN(rsum); PIN(rsq); SBAR();
        }
    };
    asm volatile("s_waitcnt vmcnt(14)\n\ts_barrier" ::: "memory");
    STAMP_NOVM(0,1);
    ldfr(0, 0, 0);
#pragma unroll
    for (int ks = 0; ks < 10; ++ks) {
        const int slot = ks % 3;
        ldfr(1, slot, 1); mm(0, true);
        if (w < 2) {
            const lds_ptr sb = lds + slot * G1_SLOT;
#pragma unroll
            for (int s2 = 0; s2 < 2; ++s2) {
                const u32x4 kx = ldsr128(sb + (kxa ^ (s2 << 6)));
                const u32x4 kw = ldstr2(sb + kwa + s2 * 1280, sb + kwa + s2 * 1280 + 160);
                kacc = (cg & 1) ? mfma16(kx, kw, kacc) : mfma16(kw, kx, kacc);
#pragma unroll
                for (int j = 0; j < 4; ++j) kcs = dot2(kw[j], ONES, kcs);
            }
            PIN(kcs); PIN(kacc);
        }
        ldfr(0, slot, 2); mm(1, false);
        ldfr(1, slot, 3); mm(0, false);
        if (ks < 9) {
            if (ks < 8) asm volatile("s_waitcnt vmcnt(7) lgkmcnt(0)\n\ts_barrier" ::: "memory");
            else asm volatile("s_waitcnt vmcnt(0) lgkmcnt(0)\n\ts_barrier" ::: "memory");
            if (ks + 3 < 10) stage(ks + 3, slot);
            ldfr(0, (ks + 1) % 3, 0);
        }
        mm(1, false);
    }
    STAMP(0,2);
    rsum += xhalf(rsum); rsq += xhalf(rsq);
    float m0, r0;
    {
        const float mean = rsum * (1.f / D_);
        const float var = fmaxf(rsq * (1.f / D_) - mean * mean, 0.f);
        m0 = bfr(mean);
        r0 = 1.f / bfr(__builtin_sqrtf(bfr(bfr(var) + 1e-5f)));
    }
    cs0 += xhalf(cs0); cs1 += xhalf(cs1);
    *(LAS float*)(lds + G1_CS + ((mi * 2 + ni) * 64 + lane) * 4) = h ? cs1 : cs0;
    if (h == 0) *(LAS f32x2*)(lds + G1_ST + (64 * mi + 32 * ni + t) * 8) = (f32x2){m0, r0};
    asm volatile("s_waitcnt lgkmcnt(0)\n\ts_barrier" ::: "memory");
    const f32x2 st1 = *(LAS f32x2*)(lds + G1_ST + (64 * mi + 32 * (ni ^ 1) + t) * 8);
    {
        float c = 0.f;
#pragma unroll
        for (int j = 0; j < 4; ++j) c += *(LAS float*)(lds + G1_CS + ((j * 2 + ni) * 64 + lane) * 4);
        *(LAS float*)(lds + G1_CW + (w * 64 + lane) * 4) = c;
    }
    f32x4 cw[2][4];
#pragma unroll
    for (int u = 0; u < 2; ++u)
#pragma unroll
        for (int g = 0; g < 4; ++g) cw[u][g] = *(LAS f32x4*)(lds + G1_CW + (w * 64 + u * 32 + 8 * g + 4 * h) * 4);

    if (w < 2) {
        kcs += __shfl_xor(kcs, 16); kcs += __shfl_xor(kcs, 32);
        const int tok = 256 * rg + 16 * (cg >> 1);
        char* rec = ws + WS_KV + (size_t)(tok >> 5) * REC;
        const int a = (tok >> 4) & 1;
        if (cg & 1) {
            const f32x4 sa = *(LAS f32x4*)(lds + G1_ST + (16 * (cg >> 1) + 4 * kg) * 8), sb = *(LAS f32x4*)(lds + G1_ST + (16 * (cg >> 1) + 4 * kg + 2) * 8);
            const int d = 16 * w + kt;
            u32x2 o = {pk2((kacc[0] - sa[0] * kcs) * sa[1], (kacc[1] - sa[2] * kcs) * sa[3]), pk2((kacc[2] - sb[0] * kcs) * sb[1], (kacc[3] - sb[2] * kcs) * sb[3])};
            if (d == 20) o = (u32x2){ONES, ONES};
            if (d <= 20) *(u32x2*)(rec + 1280 + a * 672 + (kg >> 1) * 336 + d * 16 + (kg & 1) * 8) = o;
        } else {
            const f32x2 s = *(LAS f32x2*)(lds + G1_ST + krow * 8);
            float y[4];
#pragma unroll
            for (int r = 0; r < 4; ++r) y[r] = (kacc[r] - s[0] * __shfl(kcs, 4 * kg + r)) * s[1];
            const unsigned o0 = pk2(y[0], y[1]), o1 = pk2(y[2], y[3]);
            const int key = 16 * a + kt;
            if (w == 0) *(u32x2*)(rec + (key + 32 * (kg >> 1)) * 16 + (kg & 1) * 8) = (u32x2){o0, o1};
            else if (kg == 0) { *(unsigned*)(rec + 1024 + key * 4) = o0; *(unsigned*)(rec + 1024 + (key + 32) * 4) = o1; }
        }
    }
#pragma unroll
    for (int tt = 0; tt < 2; ++tt) {
        const float m = tt ? st1[0] : m0, r = tt ? st1[1] : r0;
        const int T0 = 256 * rg + 64 * mi + 32 * (tt ^ ni);
        if (isq) {
            const float sc = r * (LOG2E * 0.22360679774997896f);
#pragma unroll
            for (int u = 0; u < 2; ++u) {
                char* qp = ws + WS_QG + ((size_t)(T0 >> 5) * 160 + 32 * (cg - 27) + 16 * u + 8 * ni + h) * 256 + t * 8;
#pragma unroll
                for (int g = 0; g < 4; ++g) {
                    float y[4];
#pragma unroll
                    for (int j = 0; j < 4; ++j) y[j] = (acc[tt][u][4 * g + j] - m * cw[u][g][j]) * sc;
                    *(u32x2*)(qp + g * 512) = (u32x2){pk2(y[0], y[1]), pk2(y[2], y[3])};
                }
            }
        } else {
            const float rl = -r * LOG2E, ir2 = 1.f / (r * r);
            char* hp = ws + WS_HF + ((size_t)((T0 >> 4) + (t >> 4)) * 54 + 2 * cg + ni) * 1024 + (h * 16 + (t & 15)) * 16;
#pragma unroll
            for (int a = 0; a < 2; ++a) {
                float y[8];
#pragma unroll
                for (int j = 0; j < 8; ++j) {
                    const int rr = 8 * a + j;
                    const float zg = acc[tt][0][rr] - m * cw[0][rr >> 2][rr & 3];
                    const float zu = acc[tt][1][rr] - m * cw[1][rr >> 2][rr & 3];
                    const float e = __builtin_amdgcn_exp2f(zg * rl);
                    y[j] = zg * zu * __builtin_amdgcn_rcpf(e * ir2 + ir2);
                }
                *(u32x4*)(hp + a * 512) = (u32x4){pk2(y[0], y[1]), pk2(y[2], y[3]), pk2(y[4], y[5]), pk2(y[6], y[7])};
            }
        }
    }
    STAMP(0,3); STAMP_FLUSH(0,1);
}

struct KVf { u32x4 kh; u32x2 kl; u32x4 v0, v1; };
DEVI f32x16 qk_hi(u32x4 k, u32x4 q) { return mfma32(k, q, (f32x16){}); }
DEVI f32x16 qk_lo(u32x2 k, u32x2 q, f32x16 c) { return __builtin_amdgcn_mfma_f32_32x32x8bf16_1k(__builtin_bit_cast(s16x4, k), __builtin_bit_cast(s16x4, q), c, 0, 0, 0); }
template <int HALF> DEVI f32x16 pv(u32x4 v, const f32x16& p, f32x16 o, int idx) {
    f32x8 hh = HALF ? __builtin_shufflevector(p, p, 8, 9, 10, 11, 12, 13, 14, 15) : __builtin_shufflevector(p, p, 0, 1, 2, 3, 4, 5, 6, 7);
    return __builtin_amdgcn_smfmac_f32_32x32x32_bf16(__builtin_bit_cast(bf16x8, v), __builtin_bit_cast(bf16x16, hh), o, idx, 0, 0);
}
#define EX4(S, i) do { S[i] = __builtin_amdgcn_exp2f(S[i]); S[i + 1] = __builtin_amdgcn_exp2f(S[i + 1]); S[i + 2] = __builtin_amdgcn_exp2f(S[i + 2]); S[i + 3] = __builtin_amdgcn_exp2f(S[i + 3]); SBAR(); } while (0)
#define TILE(SC, SN, SNN, SP, KNl, QN, KNNh, QNN, VP1, OP, VC0, OC) do { \
    SN = qk_lo(KNl, ql[QN], SN); SBAR(); EX4(SC, 0); \
    OP = pv<1>(VP1, SP, OP, idx); SBAR(); EX4(SC, 4); \
    SNN = qk_hi(KNNh, qh[QNN]); SBAR(); EX4(SC, 8); \
    OC = pv<0>(VC0, SC, OC, idx); SBAR(); EX4(SC, 12); } while (0)

constexpr int K2_GRING = 0, K2_GSLOT = 9216, K2_GW = 3 * K2_GSLOT;
constexpr int K2_AO = 4 * K2_GW;
constexpr int K2_FLAG = K2_AO + 4 * 6144;
constexpr int K2_LDS = K2_FLAG + 64;

__global__ __launch_bounds__(512, 2) void k2_attn_down(const u16* __restrict__ wd, char* __restrict__ ws) {
    extern __shared__ __attribute__((aligned(16))) char smem[];
    const lds_ptr lds = (lds_ptr)smem;
    const unsigned lds0 = (unsigned)(size_t)lds;
    const int lane = threadIdx.x & 63, w = __builtin_amdgcn_readfirstlane(threadIdx.x >> 6);
    const int b = blockIdx.x;
    STAMP(1,0);
    asm volatile("s_waitcnt vmcnt(0)":::"memory");
    if (threadIdx.x == 0) *(LAS int*)(lds + K2_FLAG) = 0;
    if (w < 4) {
        __builtin_amdgcn_s_setprio(3);
        const int tq = b >> 2, hd0 = (b & 3) * 8 + 4 * (w >> 1), kh = w & 1;
        const int t = lane & 31, h = lane >> 5;
        u32x4 qh[4]; u32x2 ql[4];
#pragma unroll
        for (int i = 0; i < 4; ++i) {
            const char* qp = ws + WS_QG + ((size_t)tq * 160 + 5 * (hd0 + i)) * 256 + t * 8;
            const u32x2 a = *(const u32x2*)(qp + 512 * h), c = *(const u32x2*)(qp + 512 * h + 256);
            qh[i] = (u32x4){a.x, a.y, c.x, c.y};
            ql[i] = (u32x2){*(const unsigned*)(qp + 1024 + 4 * h), 0u};
        }
        const rsrc_t rs = mkrsrc(ws + WS_KV, 64 * REC);
        const unsigned o16 = lane * 16, o4 = 1024 + lane * 4, ov = 1280 + h * 336 + min(t, 20) * 16;
        int idx = 0xDDDD; PIN(idx);
#define LD128(vo, so) __builtin_amdgcn_raw_buffer_load_b128(rs, vo, so, 0)
#define LD32(vo, so) __builtin_amdgcn_raw_buffer_load_b32(rs, vo, so, 0)
        for (int attempt = 0;; ++attempt) {
            KVf A, B; A.kl.y = ONES; B.kl.y = ONES;
            int pk = kh * 32 * REC;
            A.kh = LD128(o16, pk); A.kl.x = LD32(o4, pk); A.v0 = LD128(ov, pk); A.v1 = LD128(ov + 672, pk);
            B.kh = LD128(o16, pk + REC); B.kl.x = LD32(o4, pk + REC);
            B.v1 = (u32x4){0, 0, 0, 0};
            f32x16 O0 = {}, O1 = {}, O2 = {}, O3 = {}, S0, S1, S2, S3 = {};
            S0 = qk_lo(A.kl, ql[0], qk_hi(A.kh, qh[0])); S1 = qk_hi(A.kh, qh[1]);
            PIN(A.v0); PIN(A.v1); PIN(B.kh); PIN(B.kl);
            pk += 2 * REC;
            STAMP(1,1);
            for (int kb = 0; kb < 32; kb += 2) {
                TILE(S0, S1, S2, S3, A.kl, 1, A.kh, 2, B.v1, O3, A.v0, O0);
                B.v0 = LD128(ov, pk - REC); B.v1 = LD128(ov + 672, pk - REC); SBAR();
                TILE(S1, S2, S3, S0, A.kl, 2, A.kh, 3, A.v1, O0, A.v0, O1);
                A.kh = LD128(o16, pk); SBAR();
                TILE(S2, S3, S0, S1, A.kl, 3, B.kh, 0, A.v1, O1, A.v0, O2);
                A.kl.x = LD32(o4, pk); SBAR();
                TILE(S3, S0, S1, S2, B.kl, 0, B.kh, 1, A.v1, O2, A.v0, O3);
                TILE(S0, S1, S2, S3, B.kl, 1, B.kh, 2, A.v1, O3, B.v0, O0);
                A.v0 = LD128(ov, pk); A.v1 = LD128(ov + 672, pk); SBAR();
                TILE(S1, S2, S3, S0, B.kl, 2, B.kh, 3, B.v1, O0, B.v0, O1);
                B.kh = LD128(o16, pk + REC); SBAR();
                TILE(S2, S3, S0, S1, B.kl, 3, A.kh, 0, B.v1, O1, B.v0, O2);
                B.kl.x = LD32(o4, pk + REC); SBAR();
                TILE(S3, S0, S1, S2, A.kl, 0, A.kh, 1, B.v1, O2, B.v0, O3);
                pk += 2 * REC;
            }
            O3 = pv<1>(B.v1, S3, O3, idx);
            STAMP(1,2);
            {
                const lds_ptr op = lds + K2_AO + w * 6144 + lane * 16;
                const f32x16& X0 = kh ? O0 : O2; const f32x16& X1 = kh ? O1 : O3;
#pragma unroll
                for (int g = 0; g < 3; ++g) {
                    *(LAS f32x4*)(op + g * 1024) = (f32x4){X0[4 * g], X0[4 * g + 1], X0[4 * g + 2], X0[4 * g + 3]};
                    *(LAS f32x4*)(op + 3072 + g * 1024) = (f32x4){X1[4 * g], X1[4 * g + 1], X1[4 * g + 2], X1[4 * g + 3]};
                }
            }
            asm volatile("s_waitcnt lgkmcnt(0)\n\ts_barrier" ::: "memory");
            bool bad = false;
#pragma unroll
            for (int i = 0; i < 2; ++i) {
                const f32x16& Y = kh ? (i ? O3 : O2) : (i ? O1 : O0);
                const lds_ptr ip = lds + K2_AO + (w ^ 1) * 6144 + i * 3072 + lane * 16;
                f32x4 z[3];
#pragma unroll
                for (int g = 0; g < 3; ++g) z[g] = *(LAS f32x4*)(ip + g * 1024) + (f32x4){Y[4 * g], Y[4 * g + 1], Y[4 * g + 2], Y[4 * g + 3]};
                const float l = __shfl(z[2][0], 32 + t);
                const float il = 1.f / l;
                float mx = 0.f;
#pragma unroll
                for (int j = 0; j < 4; ++j) { mx = fmaxf(mx, fmaxf(fabsf(z[0][j]), fabsf(z[1][j]))); if (h == 0) mx = fmaxf(mx, fabsf(z[2][j])); }
                bad |= !(l >= 9.5367431640625e-07f && l <= 1.2676506002282294e30f && mx <= 3.0e38f);
                const int G0 = 5 * (hd0 + 2 * kh + i) + h;
                char* ob = ws + WS_OF + (size_t)(2 * tq + (t >> 4)) * 20480 + (t & 15) * 16;
#pragma unroll
                for (int g = 0; g < 3; ++g) {
                    const int G = G0 + 2 * g;
                    if (g < 2 || h == 0) *(u32x2*)(ob + (G >> 1) * 256 + (G & 1) * 8) = (u32x2){pk2(z[g][0] * il, z[g][1] * il), pk2(z[g][2] * il, z[g][3] * il)};
                }
            }
            if (attempt == 0 && __any(bad) && lane == 0) *(LAS int*)(lds + K2_FLAG) = 1;
            asm volatile("s_waitcnt lgkmcnt(0)\n\ts_barrier" ::: "memory");
            STAMP(1,3);
            if (attempt == 1 || __builtin_amdgcn_readfirstlane(*(volatile LAS int*)(lds + K2_FLAG)) == 0) break;
            float mxs[4];
#pragma unroll
            for (int i = 0; i < 4; ++i) mxs[i] = -__builtin_inff();
            for (int kb = 0; kb < 64; ++kb) {
                const u32x4 kh4 = LD128(o16, kb * REC); const u32x2 kl2 = {LD32(o4, kb * REC), ONES};
#pragma unroll
                for (int i = 0; i < 4; ++i) {
                    const f32x16 s = qk_lo(kl2, ql[i], qk_hi(kh4, qh[i]));
#pragma unroll
                    for (int r = 0; r < 16; ++r) mxs[i] = fmaxf(mxs[i], s[r]);
                }
            }
#pragma unroll
            for (int i = 0; i < 4; ++i) {
                const float m = -fmaxf(mxs[i], xhalf(mxs[i]));
                const float a0 = bfr(m), a1 = bfr(m - a0), a2 = bfr(m - a0 - a1);
                ql[i].y = h ? pk2(a2, 0.f) : pk2(a0, a1);
            }
        }
    } else {
        const int gw = w - 4, xcd = b & 7, bi = b >> 3;
        const int rt = 8 * (xcd & 3) + (bi & 7), ct = 4 * (xcd >> 2) + (bi >> 3);
        const int ks0 = gw * 13 + min(gw, 2), n = gw < 2 ? 14 : 13;
        const rsrc_t rh = mkrsrc(ws + WS_HF, 128 * 54 * 1024), rw = mkrsrc(wd, F_ * D_ * 2);
        unsigned vw[5];
#pragma unroll
        for (int i = 0; i < 5; ++i) { const int c = i * 64 + lane; vw[i] = (c / 10) * 1280 + (c % 10) * 16; }
        const unsigned vh = lane * 16;
        const unsigned lb = lds0 + gw * K2_GW;
        auto stage = [&](int ks, int slot) {
#pragma unroll
            for (int i = 0; i < 4; ++i) dma16(lb + slot * K2_GSLOT + i * 1024, vh, rh, ((4 * rt + i) * 54 + ks) * 1024);
#pragma unroll
            for (int i = 0; i < 5; ++i) dma16(lb + slot * K2_GSLOT + 4096 + i * 1024, vw[i], rw, ks * 32 * 1280 + ct * 160);
        };
        stage(ks0, 0); stage(ks0 + 1, 1); stage(ks0 + 2, 2);
        const int g = lane >> 4;
        const int wa = gw * K2_GW + 4096 + (16 * (g >> 1) + 4 * (g & 1) + ((lane & 15) >> 2)) * 160 + (lane & 3) * 8;
        const int ha = gw * K2_GW + lane * 16;
        f32x4 acc[5][4] = {};
        int slot = 0;
        STAMP_NOVM(1,1);
        for (int i = 0; i < n; ++i) {
            asm volatile("s_waitcnt vmcnt(18)" ::: "memory");
            u32x4 hf[4], wf[5];
            const lds_ptr sb = lds + slot * K2_GSLOT;
#pragma unroll
            for (int j = 0; j < 4; ++j) hf[j] = ldsr128(sb + ha + j * 1024);
#pragma unroll
            for (int j = 0; j < 5; ++j) wf[j] = ldstr2(sb + wa + j * 32, sb + wa + j * 32 + 8 * 160);
            asm volatile("s_waitcnt lgkmcnt(0)" ::: "memory");
#pragma unroll
            for (int j = 0; j < 4; ++j) PIN(hf[j]);
#pragma unroll
            for (int j = 0; j < 5; ++j) PIN(wf[j]);
            stage(min(ks0 + i + 3, 53), slot);
#pragma unroll
            for (int nt = 0; nt < 5; ++nt)
#pragma unroll
                for (int tt = 0; tt < 4; ++tt) acc[nt][tt] = mfma16(wf[nt], hf[tt], acc[nt][tt]);
            slot = slot == 2 ? 0 : slot + 1;
        }
        asm volatile("s_waitcnt vmcnt(0)" ::: "memory");
        STAMP(1,2); STAMP_FLUSH(1,1);
#pragma unroll
        for (int nt = 0; nt < 5; ++nt)
#pragma unroll
            for (int tt = 0; tt < 4; ++tt) *(LAS f32x4*)(lds + gw * K2_GW + (nt * 4 + tt) * 1024 + lane * 16) = acc[nt][tt];
        asm volatile("s_waitcnt lgkmcnt(0)\n\ts_barrier" ::: "memory");
#pragma unroll
        for (int j = 0; j < 5; ++j) {
            const int tile = gw * 5 + j;
            f32x4 d = *(LAS f32x4*)(lds + tile * 1024 + lane * 16);
#pragma unroll
            for (int k = 1; k < 4; ++k) d += *(LAS f32x4*)(lds + k * K2_GW + tile * 1024 + lane * 16);
            *(f32x4*)(ws + WS_DN + ((size_t)b * 20 + tile) * 1024 + lane * 16) = d;
        }
        asm volatile("s_waitcnt lgkmcnt(0)\n\ts_barrier" ::: "memory");
        STAMP(1,3);
        if (__builtin_amdgcn_readfirstlane(*(volatile LAS int*)(lds + K2_FLAG)) != 0) {
            asm volatile("s_barrier" ::: "memory");
            asm volatile("s_barrier" ::: "memory");
        }
    }
}

constexpr int K3_W = 25600, K3_LDS = 4 * K3_W;
__global__ __launch_bounds__(256, 1) void k3_out(const u16* __restrict__ x, const u16* __restrict__ wo, const char* __restrict__ ws, u16* __restrict__ out) {
    extern __shared__ __attribute__((aligned(16))) char smem[];
    const lds_ptr lds = (lds_ptr)smem;
    const unsigned lds0 = (unsigned)(size_t)lds;
    const int lane = threadIdx.x & 63, gw = __builtin_amdgcn_readfirstlane(threadIdx.x >> 6);
    const int b = blockIdx.x, xcd = b & 7, bi = b >> 3;
    const int rt = 8 * (xcd & 3) + (bi & 7), ct = 4 * (xcd >> 2) + (bi >> 3);
    STAMP(2,0);
    const rsrc_t rw = mkrsrc(wo, D_ * D_ * 2);
    unsigned vw[5];
#pragma unroll
    for (int i = 0; i < 5; ++i) { const int c = i * 64 + lane; vw[i] = (c / 10) * 1280 + (c % 10) * 16; }
#pragma unroll
    for (int s = 0; s < 5; ++s)
#pragma unroll
        for (int i = 0; i < 5; ++i) dma16(lds0 + gw * K3_W + s * 5120 + i * 1024, vw[i], rw, (gw * 5 + s) * 32 * 1280 + ct * 160);
    u32x4 of[5][4];
#pragma unroll
    for (int s = 0; s < 5; ++s)
#pragma unroll
        for (int j = 0; j < 4; ++j) of[s][j] = *(const u32x4*)(ws + WS_OF + ((size_t)(4 * rt + j) * 20 + gw * 5 + s) * 1024 + lane * 16);
    f32x4 dn[5]; u32x2 xv[5];
    const int tl = lane & 15, g = lane >> 4;
#pragma unroll
    for (int j = 0; j < 5; ++j) {
        const int tile = gw * 5 + j, nt = tile >> 2, tt = tile & 3;
        dn[j] = *(const f32x4*)(ws + WS_DN + ((size_t)b * 20 + tile) * 1024 + lane * 16);
        xv[j] = *(const u32x2*)(x + (size_t)(64 * rt + 16 * tt + tl) * D_ + 80 * ct + 16 * nt + 4 * g);
    }
    const int wa = gw * K3_W + (8 * g + ((lane & 15) >> 2)) * 160 + (lane & 3) * 8;
    f32x4 acc[5][4] = {};
    asm volatile("s_waitcnt vmcnt(0)" ::: "memory");
    STAMP(2,1);
#pragma unroll
    for (int s = 0; s < 5; ++s) {
        u32x4 wf[5];
#pragma unroll
        for (int j = 0; j < 5; ++j) wf[j] = ldstr2(lds + wa + s * 5120 + j * 32, lds + wa + s * 5120 + j * 32 + 4 * 160);
#pragma unroll
        for (int nt = 0; nt < 5; ++nt)
#pragma unroll
            for (int tt = 0; tt < 4; ++tt) acc[nt][tt] = mfma16(wf[nt], of[s][tt], acc[nt][tt]);
    }
    asm volatile("s_waitcnt lgkmcnt(0)" ::: "memory");
#pragma unroll
    for (int nt = 0; nt < 5; ++nt)
#pragma unroll
        for (int tt = 0; tt < 4; ++tt) *(LAS f32x4*)(lds + gw * K3_W + (nt * 4 + tt) * 1024 + lane * 16) = acc[nt][tt];
    asm volatile("s_waitcnt lgkmcnt(0)\n\ts_barrier" ::: "memory");
#pragma unroll
    for (int j = 0; j < 5; ++j) {
        const int tile = gw * 5 + j, nt = tile >> 2, tt = tile & 3;
        f32x4 s = *(LAS f32x4*)(lds + tile * 1024 + lane * 16);
#pragma unroll
        for (int k = 1; k < 4; ++k) s += *(LAS f32x4*)(lds + k * K3_W + tile * 1024 + lane * 16);
        float y[4];
#pragma unroll
        for (int r = 0; r < 4; ++r) {
            const float d = dn[j][r];
            const float gt = d * __builtin_amdgcn_rcpf(1.f + __builtin_amdgcn_exp2f(-d * LOG2E));
            const unsigned xw = xv[j][r >> 1];
            const float xf = __uint_as_float((r & 1) ? (xw & 0xffff0000u) : (xw << 16));
            y[r] = xf + s[r] * gt;
        }
        *(u32x2*)(out + (size_t)(64 * rt + 16 * tt + tl) * D_ + 80 * ct + 16 * nt + 4 * g) = (u32x2){pk2(y[0], y[1]), pk2(y[2], y[3])};
    }
    STAMP(2,2);
}

extern "C" void kernel_launch(void* const* d_in, const int* in_sizes, int n_in, void* d_out, int out_size, void* d_ws, size_t ws_size, hipStream_t stream) {
    const u16* x = (const u16*)d_in[0]; const u16* wq = (const u16*)d_in[1]; const u16* wk = (const u16*)d_in[2]; const u16* wv = (const u16*)d_in[3];
    const u16* wo = (const u16*)d_in[4]; const u16* wg = (const u16*)d_in[6]; const u16* wu = (const u16*)d_in[7]; const u16* wd = (const u16*)d_in[8];
    (void)hipFuncSetAttribute((const void*)k1_proj, hipFuncAttributeMaxDynamicSharedMemorySize, G1_LDS);
    (void)hipFuncSetAttribute((const void*)k2_attn_down, hipFuncAttributeMaxDynamicSharedMemorySize, K2_LDS);
    (void)hipFuncSetAttribute((const void*)k3_out, hipFuncAttributeMaxDynamicSharedMemorySize, K3_LDS);
    k1_proj<<<256, 512, G1_LDS, stream>>>(x, wq, wk, wv, wg, wu, (char*)d_ws);
    k2_attn_down<<<256, 512, K2_LDS, stream>>>(wd, (char*)d_ws);
    k3_out<<<256, 256, K3_LDS, stream>>>(x, wo, (const char*)d_ws, (u16*)d_out);
}
```

```cpp
#include <hip/hip_runtime.h>
#include <hip/hip_bf16.h>

typedef unsigned short u16;
typedef unsigned long long u64;
typedef __bf16 bf16x2 __attribute__((ext_vector_type(2)));
typedef __bf16 bf16x8 __attribute__((ext_vector_type(8)));
typedef __bf16 bf16x16 __attribute__((ext_vector_type(16)));
typedef short s16x4 __attribute__((ext_vector_type(4)));
typedef float f32x16 __attribute__((ext_vector_type(16)));
typedef float f32x8 __attribute__((ext_vector_type(8)));
typedef float f32x4 __attribute__((ext_vector_type(4)));
typedef float f32x2 __attribute__((ext_vector_type(2)));
typedef unsigned u32x4 __attribute__((ext_vector_type(4)));
typedef unsigned u32x2 __attribute__((ext_vector_type(2)));
typedef __amdgpu_buffer_rsrc_t rsrc_t;
#define LAS __attribute__((address_space(3)))
typedef LAS char* lds_ptr;

#define SBAR() __builtin_amdgcn_sched_barrier(0)
#define PIN(x) asm volatile("" : "+v"(x))
#define DEVI __device__ __forceinline__

constexpr int S_ = 2048, D_ = 640, F_ = 1728;
constexpr int REC = 5760;
constexpr unsigned WS_EP = 0;
constexpr unsigned WS_F0 = 4096;
constexpr unsigned WS_CK = 8192;
constexpr unsigned WS_C1 = 12288;
constexpr unsigned WS_RK = 303104, WS_R1 = 311296;
constexpr unsigned WS_ST = 16384;
constexpr unsigned WS_F1 = 32768, WS_F2 = 65536;
constexpr unsigned WS_KV = 98304;
constexpr unsigned WS_QG = 327680;
constexpr unsigned WS_HF = WS_QG + 64 * 80 * 512;
constexpr unsigned WS_OF = WS_HF + 128 * 54 * 1024;
constexpr unsigned WS_WF = WS_OF + 128 * 20 * 1024;
constexpr unsigned WS_END = WS_WF + 74 * 40 * 1024;
constexpr int SC1 = 16;

#define STAMP_DECL
#define STAMP(i)
#define FLUSH(i)

DEVI rsrc_t mkrsrc(const void* p, unsigned bytes) { return __builtin_amdgcn_make_buffer_rsrc((void*)p, 0, bytes, 0x00020000); }
DEVI void dma16(unsigned lds, unsigned voff, rsrc_t rs, unsigned soff) {
    asm volatile("s_mov_b32 m0, %0\n\ts_nop 0\n\tbuffer_load_dwordx4 %1, %2, %3 offen lds" ::"s"(lds), "v"(voff), "s"(rs), "s"(soff) : "memory");
}
DEVI void dma4(unsigned lds, unsigned voff, rsrc_t rs, unsigned soff) {
    asm volatile("s_mov_b32 m0, %0\n\ts_nop 0\n\tbuffer_load_dword %1, %2, %3 offen lds" ::"s"(lds), "v"(voff), "s"(rs), "s"(soff) : "memory");
}
DEVI u32x4 ld128_raw(unsigned voff, rsrc_t rs, unsigned soff) {
    u32x4 r; asm volatile("buffer_load_dwordx4 %0, %1, %2, %3 offen" : "=v"(r) : "v"(voff), "s"(rs), "s"(soff) : "memory"); return r;
}
DEVI u32x4 ldsr128(lds_ptr p) { return *(LAS u32x4*)p; }
DEVI f32x4 ldsf4(lds_ptr p) { return *(LAS f32x4*)p; }
DEVI float ldsf(lds_ptr p) { return *(LAS float*)p; }
DEVI u32x2 ldstr(lds_ptr p) { return __builtin_bit_cast(u32x2, __builtin_amdgcn_ds_read_tr16_b64_v4i16((LAS s16x4*)p)); }
DEVI u32x4 ldstr2(lds_ptr p0, lds_ptr p1) { u32x2 a = ldstr(p0), b = ldstr(p1); return (u32x4){a.x, a.y, b.x, b.y}; }
DEVI f32x16 mfma32(u32x4 a, u32x4 b, f32x16 c) { return __builtin_amdgcn_mfma_f32_32x32x16_bf16(__builtin_bit_cast(bf16x8, a), __builtin_bit_cast(bf16x8, b), c, 0, 0, 0); }
DEVI f32x4 mfma16(u32x4 a, u32x4 b, f32x4 c) { return __builtin_amdgcn_mfma_f32_16x16x32_bf16(__builtin_bit_cast(bf16x8, a), __builtin_bit_cast(bf16x8, b), c, 0, 0, 0); }
DEVI unsigned pk2(float lo, float hi) { bf16x2 v = {(__bf16)lo, (__bf16)hi}; return __builtin_bit_cast(unsigned, v); }
DEVI float bfr(float x) { return (float)(__bf16)x; }
DEVI float bflo(unsigned v) { return __uint_as_float(v << 16); }
DEVI float bfhi(unsigned v) { return __uint_as_float(v & 0xffff0000u); }
constexpr unsigned ONES = 0x3f803f80u;
constexpr float LOG2E = 1.4426950408889634f;
constexpr unsigned OOB = 0x7fffff00u;

template <int SLEEP> DEVI void wait_flags(rsrc_t rws, unsigned off, bool act, u32x2 tag) {
#pragma unroll 1
    for (int spin = 0; spin < (1 << 18); ++spin) {
        const u32x2 v = __builtin_amdgcn_raw_buffer_load_b64(rws, off, 0, SC1);
        if (__all(!act || (v.x == tag.x && v.y == tag.y))) break;
        __builtin_amdgcn_s_sleep(SLEEP);
    }
    asm volatile("" ::: "memory");
}
DEVI void lds_wait(lds_ptr p, int n) {
#pragma unroll 1
    for (int spin = 0; spin < (1 << 22); ++spin) {
        if (__builtin_amdgcn_readfirstlane(*(volatile LAS int*)p) >= n) break;
        __builtin_amdgcn_s_sleep(2);
    }
    asm volatile("" ::: "memory");
}
DEVI int lds_inc(lds_ptr p) { return __hip_atomic_fetch_add((LAS int*)p, 1, __ATOMIC_RELAXED, __HIP_MEMORY_SCOPE_WORKGROUP); }

DEVI void arrive(char* ws, rsrc_t rws, unsigned cnt, unsigned rdy, int lane, u32x2 tag) {
    unsigned old = 0;
    if (lane == 0) old = __hip_atomic_fetch_add((__attribute__((address_space(1))) unsigned*)(ws + cnt), 1u, __ATOMIC_RELAXED, __HIP_MEMORY_SCOPE_AGENT);
    if (__builtin_amdgcn_readfirstlane(old) == 31u) __builtin_amdgcn_raw_buffer_store_b64(tag, rws, lane < 8 ? rdy + lane * 128 : OOB, 0, SC1);
}
DEVI float wave_sum(float v) {
#define DPP_ADD(ctrl, rm) v += __uint_as_float(__builtin_amdgcn_update_dpp(0u, __float_as_uint(v), ctrl, rm, 0xf, true))
    DPP_ADD(0x111, 0xf); DPP_ADD(0x112, 0xf); DPP_ADD(0x114, 0xf); DPP_ADD(0x118, 0xf); DPP_ADD(0x142, 0xa); DPP_ADD(0x143, 0xc);
    return __uint_as_float(__builtin_amdgcn_readlane(__float_as_uint(v), 63));
}
DEVI void dma16_lo40(unsigned lds, unsigned voff, rsrc_t rs, unsigned soff) {
    asm volatile("s_mov_b32 m0, %0\n\ts_mov_b32 exec_hi, 0xff\n\tbuffer_load_dwordx4 %1, %2, %3 offen lds\n\ts_mov_b32 exec_hi, -1" ::"s"(lds), "v"(voff), "s"(rs), "s"(soff) : "memory");
}

constexpr int G1_W = 32768, G1_KW = 49152, G1_SLOT = 51776;
constexpr int L_X = 3 * G1_SLOT;
constexpr int L_CS = L_X;
constexpr int L_KP = 131072;
constexpr int L_JUNK = L_CS + 1024;
constexpr int L_CNT = L_JUNK + 256;
constexpr int LDS_BYTES = 163840;
constexpr int L_RT = 65536;
constexpr int PT = 5376, GR_W = 4 * PT;
constexpr int L_AO = 102400;

constexpr unsigned CODE_BYTES = 27648;
#define QSLEEP 2
#define GSLEEP 4
#define HDELAY __builtin_amdgcn_s_sleep(16)
#define HPRIO 0
#define CPRIO 2
#define APRIO 2
#define GPRIO 0
#define GPACE
struct KVf { u32x4 k0, k1, kl, v0, v1, v2, v3; };
DEVI f32x16 qk_hi(u32x4 k, u32x4 q) { return mfma32(k, q, (f32x16){}); }
template <int B> DEVI f32x16 qk_lo(u32x4 k, u32x2 q, f32x16 c) {
    const u32x2 kk = B ? __builtin_shufflevector(k, k, 2, 3) : __builtin_shufflevector(k, k, 0, 1);
    return __builtin_amdgcn_mfma_f32_32x32x8bf16_1k(__builtin_bit_cast(s16x4, kk), __builtin_bit_cast(s16x4, q), c, 0, 0, 0);
}
template <int HALF> DEVI f32x16 pv(u32x4 v, const f32x16& p, f32x16 o, int idx) {
    f32x8 hh = HALF ? __builtin_shufflevector(p, p, 8, 9, 10, 11, 12, 13, 14, 15) : __builtin_shufflevector(p, p, 0, 1, 2, 3, 4, 5, 6, 7);
    return __builtin_amdgcn_smfmac_f32_32x32x32_bf16(__builtin_bit_cast(bf16x8, v), __builtin_bit_cast(bf16x16, hh), o, idx, 0, 0);
}
#define EX4(S, i) do { S[i] = __builtin_amdgcn_exp2f(S[i]); S[i + 1] = __builtin_amdgcn_exp2f(S[i + 1]); S[i + 2] = __builtin_amdgcn_exp2f(S[i + 2]); S[i + 3] = __builtin_amdgcn_exp2f(S[i + 3]); SBAR(); } while (0)
#define TILE(SC, SN, SNN, SP, KB, KNl, QN, KNNh, QNN, VP1, OP, VC0, OC) do { \
    SN = qk_lo<KB>(KNl, ql[QN], SN); SBAR(); EX4(SC, 0); \
    OP = pv<1>(VP1, SP, OP, idx); SBAR(); EX4(SC, 4); \
    SNN = qk_hi(KNNh, qh[QNN]); SBAR(); EX4(SC, 8); \
    OC = pv<0>(VC0, SC, OC, idx); SBAR(); EX4(SC, 12); } while (0)

__global__ __launch_bounds__(512, 2) void fused_block(const u16* __restrict__ x, const u16* __restrict__ wq, const u16* __restrict__ wk, const u16* __restrict__ wv,
                                                       const u16* __restrict__ wo, const u16* __restrict__ wg, const u16* __restrict__ wu, const u16* __restrict__ wd,
                                                       char* __restrict__ ws, u16* __restrict__ out) {
    extern __shared__ __attribute__((aligned(16))) char smem[];
    const lds_ptr lds = (lds_ptr)smem;
    const unsigned lds0 = (unsigned)(size_t)lds;
    const int lane = threadIdx.x & 63, w = __builtin_amdgcn_readfirstlane(threadIdx.x >> 6);
    const int b = blockIdx.x, xcd = b & 7, bi = b >> 3;
    STAMP_DECL;
    u64 code0;
    asm volatile("s_getpc_b64 %0" : "=s"(code0));
    STAMP(0);
    const rsrc_t rws = mkrsrc(ws, WS_END);
    const rsrc_t rx = mkrsrc(x, S_ * D_ * 2);
    const int l15 = lane & 15, g4 = lane >> 4;

    const int rg = 4 * (xcd & 1) + (bi & 3), cg = 4 * (bi >> 2) + (xcd >> 1);
    const bool isq = cg >= 27;
    const unsigned vl16 = lane * 16;
    const int t = lane & 31, h = lane >> 5;
    u32x2 tag;
    auto make_tag = [&]() {
        u64 epoch;
        asm volatile("s_load_dwordx2 %0, %1, 0x0\n\ts_waitcnt lgkmcnt(0)" : "=&s"(epoch) : "s"(ws) : "memory");
        u64 z = epoch + 0x9E3779B97F4A7C15ull;
        z = (z ^ (z >> 30)) * 0xBF58476D1CE4E5B9ull; z = (z ^ (z >> 27)) * 0x94D049BB133111EBull; z ^= z >> 31;
        tag = (u32x2){(unsigned)z, (unsigned)(z >> 32)};
    };
    const int un = min(3 * b + max((w & 3) - 1, 0), 739), us = un / 10;
    auto rt_off = [&](int g, int sl) {
        const int C = 4 * (us - 54) + g, pp = (C * 205) >> 10, c = C - 5 * pp;
        return ((us < 54 ? 32 * us + 16 * (g >> 1) + 4 * (g & 1) + 8 * (sl >> 2) : 40 * pp + (c == 4 ? 16 + 20 * (sl >> 2) : (c >> 1) * 20 + (c & 1) * 4 + 8 * (sl >> 2))) + (sl & 3)) * 1280 + (un - 10 * us) * 128;
    };
    if (w < 4) {
        __builtin_amdgcn_s_setprio(HPRIO);
        const bool isv = cg & 1;
        const rsrc_t rw = isq ? mkrsrc(wq, D_ * D_ * 2) : mkrsrc((w >> 1) ? wu : wg, D_ * F_ * 2);
        const rsrc_t rkv = mkrsrc(isv ? wv : wk, D_ * 20 * 2);
        const unsigned pitch = isq ? D_ * 2 : F_ * 2;
        const unsigned vx0 = (lane >> 3) * 1280 + (((lane & 7) ^ g4) << 4);
        const unsigned vx1 = (lane >> 3) * 1280 + (((lane & 7) ^ (4 + g4)) << 4);
        const unsigned vw = (lane >> 3) * pitch + (((lane & 7) ^ ((g4 & 1) << 2)) << 4);
        const unsigned sx = (256 * rg + 64 * w) * 1280, sw = 32 * (w & 1) * pitch + (isq ? (cg - 27) * 256 + (w >> 1) * 128 : cg * 128);
        auto dma = [&](int ks, unsigned lb, int i) {
            if (i == 12) dma16_lo40(lb + G1_KW + w * 640, vl16, rkv, ks * 2560 + w * 640);
            else if (i % 3 == 2) dma16(lb + G1_W + w * 4096 + (i / 3) * 1024, vw, rw, sw + (ks * 64 + (i / 3) * 8) * pitch);
            else { const int j = i - i / 3; dma16(lb + w * 8192 + j * 1024, (j & 1) ? vx1 : vx0, rx, sx + j * 8 * 1280 + ks * 128); }
        };
#pragma unroll
        for (int i = 0; i < 13; ++i) dma(0, lds0, i);
#pragma unroll
        for (int i = 0; i < 13; ++i) dma(1, lds0 + G1_SLOT, i);
        STAMP(12);
        const int ca = G1_W + w * 4096 + (lane >> 3) * 128 + (((lane & 7) ^ ((g4 & 1) << 2)) << 4);
        const int kxa = (16 * (cg >> 1) + l15) * 128 + (((4 * (w & 1) + g4) ^ (l15 >> 1)) << 4);
        const int kwa = G1_KW + (32 * (w & 1) + 8 * g4 + (l15 >> 2)) * 40 + 32 * (w >> 1) + (lane & 3) * 8;
        float cs[8] = {};
        f32x4 kp = {}, kc = {};
        u32x4 on = {ONES, ONES, ONES, ONES}; PIN(on);
        auto side = [&](int sb, bool dodma, int ks, int db, bool docs, bool dokv) {
            u32x4 cv[4], kx; u32x2 kwl, kwh;
#pragma unroll
            for (int i = 0; i < 13; ++i) {
                if (dodma) dma(ks, lds0 + db, i);
                if (i == 0 && dokv) { kwl = ldstr(lds + sb + kwa); kwh = ldstr(lds + sb + kwa + 160); kx = ldsr128(lds + sb + kxa); }
                if (i == 1 && docs) { cv[0] = ldsr128(lds + sb + ca); cv[1] = ldsr128(lds + sb + ca + 1024); cv[2] = ldsr128(lds + sb + ca + 2048); cv[3] = ldsr128(lds + sb + ca + 3072); }
                if (i >= 4 && i < 12 && docs) {
#pragma unroll
                    for (int j = 2 * (i - 4); j < 2 * (i - 4) + 2; ++j) {
                        const unsigned v = cv[j >> 2][j & 3];
                        asm volatile("v_add_f32 %0, %0, %2\n\tv_add_f32 %1, %1, %3" : "+v"(cs[2 * (j & 3)]), "+v"(cs[2 * (j & 3) + 1]) : "v"(bflo(v)), "v"(bfhi(v)));
                    }
                }
                if (i == 3 && dokv) {
                    PIN(kx); SBAR();
                    __builtin_amdgcn_s_setprio(3);
                    kp = mfma16((u32x4){kwl.x, kwl.y, kwh.x, kwh.y}, kx, kp); kc = mfma16((u32x4){kwl.x, kwl.y, kwh.x, kwh.y}, on, kc);
                    SBAR();
                    if (docs) __builtin_amdgcn_s_setprio(HPRIO);
                }
                SBAR();
            }
        };
        asm volatile("s_waitcnt vmcnt(13)\n\ts_barrier" ::: "memory");
        STAMP(1);
        int pb = 2 * G1_SLOT, sb = 0, nb = G1_SLOT;
#pragma unroll 1
        for (int ks = 0; ks < 8; ++ks) {
            if (ks == 2 && threadIdx.x == 0) __builtin_amdgcn_raw_buffer_store_b64(*(LAS u32x2*)(lds + L_CNT + 32), rws, WS_F0 + (rg * 32 + cg) * 8, 0, SC1);
            side(sb, true, ks + 2, pb, true, true);
            asm volatile("s_waitcnt vmcnt(13) lgkmcnt(0)\n\ts_barrier" ::: "memory");
            const int t_ = pb; pb = sb; sb = nb; nb = t_;
        }
        STAMP(14);
        {
            const u32x2 tv = *(LAS u32x2*)(lds + L_CNT + 32);
            tag = (u32x2){(unsigned)__builtin_amdgcn_readfirstlane(tv.x), (unsigned)__builtin_amdgcn_readfirstlane(tv.y)};
        }
        const u32x2 f0 = __builtin_amdgcn_raw_buffer_load_b64(rws, WS_F0 + (rg * 32 + t) * 8, 0, SC1);
        side(2 * G1_SLOT, false, 0, 0, true, true);
        if (!__all(f0.x == tag.x && f0.y == tag.y)) wait_flags<4>(rws, WS_F0 + (rg * 32 + t) * 8, true, tag);
        asm volatile("s_waitcnt vmcnt(0)" ::: "memory");
        const f32x2 kst = __builtin_bit_cast(f32x2, __builtin_amdgcn_raw_buffer_load_b64(rws, WS_ST + (256 * rg + 16 * (cg >> 1) + l15) * 8, 0, SC1));
        side(0, false, 0, 0, true, false);
        {
            float c4[4], c2[2];
#pragma unroll
            for (int j = 0; j < 4; ++j) { const auto r = __builtin_amdgcn_permlane32_swap(__float_as_uint(cs[j]), __float_as_uint(cs[j + 4]), false, false); c4[j] = __uint_as_float(r[0]) + __uint_as_float(r[1]); }
#pragma unroll
            for (int j = 0; j < 2; ++j) { const auto r = __builtin_amdgcn_permlane16_swap(__float_as_uint(c4[j]), __float_as_uint(c4[j + 2]), false, false); c2[j] = __uint_as_float(r[0]) + __uint_as_float(r[1]); }
#pragma unroll
            for (int j = 0; j < 2; ++j) c2[j] += __uint_as_float(__builtin_amdgcn_update_dpp(0u, __float_as_uint(c2[j]), 0x128, 0xf, 0xf, true));
            *(LAS f32x2*)(lds + L_CS + (w * 64 + (lane & 7) * 8 + 2 * g4) * 4) = (f32x2){c2[0], c2[1]};
        }
        asm volatile("s_waitcnt lgkmcnt(0)\n\ts_barrier" ::: "memory");
        side(0, false, 0, 0, false, true);
        *(LAS f32x4*)(lds + L_KP + w * 1024 + lane * 16) = (kp - kst[0] * kc) * kst[1];
        asm volatile("s_waitcnt lgkmcnt(0)" ::: "memory");
        if (lane == 0) lds_inc(lds + L_CNT + 8);
        if (w == 0) {
            lds_wait(lds + L_CNT + 8, 4);
            const int ktok = 256 * rg + 16 * (cg >> 1);
            const unsigned rec = WS_KV + (ktok >> 6) * REC; const int kq = (ktok >> 4) & 3;
            unsigned o[2][2];
#pragma unroll
            for (int c = 0; c < 2; ++c) {
                const lds_ptr kq_ = lds + L_KP + c * 2048 + lane * 16;
                const f32x4 y = ldsf4(kq_) + ldsf4(kq_ + 1024);
                o[c][0] = pk2(y[0], y[1]); o[c][1] = pk2(y[2], y[3]);
            }
            if (isv) {
                const unsigned vb = rec + 3072 + kq * 672 + (l15 >> 3) * 336 + g4 * 64 + (l15 & 7) * 2, vb1 = g4 == 0 ? vb + 256 : OOB;
                if (g4 == 1) o[1][0] = ONES;
                __builtin_amdgcn_raw_buffer_store_b16((u16)o[0][0], rws, vb, 0, SC1);
                __builtin_amdgcn_raw_buffer_store_b16((u16)(o[0][0] >> 16), rws, vb + 16, 0, SC1);
                __builtin_amdgcn_raw_buffer_store_b16((u16)o[0][1], rws, vb + 32, 0, SC1);
                __builtin_amdgcn_raw_buffer_store_b16((u16)(o[0][1] >> 16), rws, vb + 48, 0, SC1);
                __builtin_amdgcn_raw_buffer_store_b16((u16)o[1][0], rws, g4 < 2 ? vb + 256 : OOB, 0, SC1);
                __builtin_amdgcn_raw_buffer_store_b16((u16)(o[1][0] >> 16), rws, vb1 + 16, 0, SC1);
                __builtin_amdgcn_raw_buffer_store_b16((u16)o[1][1], rws, vb1 + 32, 0, SC1);
                __builtin_amdgcn_raw_buffer_store_b16((u16)(o[1][1] >> 16), rws, vb1 + 48, 0, SC1);
            } else {
                const int key = 16 * (kq & 1) + l15;
                __builtin_amdgcn_raw_buffer_store_b64((u32x2){o[0][0], o[0][1]}, rws, rec + (kq >> 1) * 1024 + (key + 32 * (g4 & 1)) * 16 + (g4 >> 1) * 8, 0, SC1);
                __builtin_amdgcn_raw_buffer_store_b64(g4 ? (u32x2){ONES, ONES} : (u32x2){o[1][0], o[1][1]}, rws, g4 < 2 ? rec + 2048 + (key + 32 * g4) * 16 + (kq >> 1) * 8 : OOB, 0, SC1);
            }
        }
        if (w >= 1) {
            __builtin_amdgcn_s_setprio(0);
            const rsrc_t rrt = us < 54 ? mkrsrc(wd, F_ * D_ * 2) : mkrsrc(wo, D_ * D_ * 2);
#pragma unroll
            for (int g = 0; g < 4; ++g) dma16(lds0 + L_RT + (w - 1) * 4096 + g * 1024, rt_off(g, lane >> 3) + (lane & 7) * 16, rrt, 0);
            __builtin_amdgcn_s_setprio(3);
            asm volatile("s_waitcnt vmcnt(0)" ::: "memory");
            const lds_ptr rp = lds + L_RT + (w - 1) * 4096 + g4 * 1024 + (l15 >> 2) * 128 + (lane & 3) * 8;
#pragma unroll
            for (int nt = 0; nt < 4; ++nt) __builtin_amdgcn_raw_buffer_store_b128(ldstr2(rp + nt * 32, rp + 512 + nt * 32), rws, WS_WF + un * 4096 + nt * 1024 + lane * 16, 0, SC1);
        }
        STAMP(2);
        __builtin_amdgcn_s_setprio(3);
        asm volatile("s_waitcnt vmcnt(0)" ::: "memory");
        if (w == 0) arrive(ws, rws, WS_CK + rg * 128, WS_RK + rg * 1024, lane, tag);
        STAMP(3);
    } else {
        const int mi = w & 1, ni = (w >> 1) & 1;
        __builtin_amdgcn_s_setprio(CPRIO);
        const rsrc_t rtouch = w == 5 ? mkrsrc(wg, D_ * F_ * 2) : w == 6 ? mkrsrc(wu, D_ * F_ * 2) : mkrsrc(wq, D_ * D_ * 2);
        if (w > 4) dma4(lds0 + L_JUNK, w == 7 ? (lane < 5 ? 64 * 1280 + b * 640 + lane * 128 : OOB) : (lane < 14 ? 64 * 3456 + b * 1728 + lane * 128 : OOB), rtouch, 0);
        {
            const int row = 256 * rg + 8 * cg + 2 * (w - 4);
            u32x4 ra[2]; unsigned rb[2];
#pragma unroll
            for (int i = 0; i < 2; ++i) { ra[i] = __builtin_amdgcn_raw_buffer_load_b128(rx, vl16, (row + i) * 1280, 0); rb[i] = __builtin_amdgcn_raw_buffer_load_b32(rx, 1024 + lane * 4, (row + i) * 1280, 0); }
            if (threadIdx.x < 264) *(LAS int*)(lds + L_CNT + lane * 4) = 0;
            if (w == 4) dma4(lds0 + L_JUNK, lane < 32 ? ((bi & 7) * 32 + lane) * 128 : OOB, mkrsrc((const void*)code0, CODE_BYTES), 0);
            make_tag();
            if (threadIdx.x == 256) *(LAS u32x2*)(lds + L_CNT + 32) = tag;
            f32x4 sv;
#pragma unroll
            for (int i = 0; i < 2; ++i) {
                float e[10];
#pragma unroll
                for (int j = 0; j < 4; ++j) { e[2 * j] = bflo(ra[i][j]); e[2 * j + 1] = bfhi(ra[i][j]); }
                e[8] = bflo(rb[i]); e[9] = bfhi(rb[i]);
                float sm = 0.f;
#pragma unroll
                for (int j = 0; j < 10; ++j) sm += e[j];
                const float mean = wave_sum(sm) * (1.f / D_);
                float sq = 0.f;
#pragma unroll
                for (int j = 0; j < 10; ++j) sq = __builtin_fmaf(e[j] - mean, e[j] - mean, sq);
                const float var = wave_sum(sq) * (1.f / D_);
                sv[2 * i] = bfr(mean); sv[2 * i + 1] = __builtin_amdgcn_rcpf(bfr(__builtin_amdgcn_sqrtf(bfr(bfr(var) + 1e-5f))));
            }
            __builtin_amdgcn_raw_buffer_store_b128(__builtin_bit_cast(u32x4, sv), rws, lane == 0 ? WS_ST + row * 8 : OOB, 0, SC1);
            __builtin_amdgcn_raw_buffer_store_b32(0u, rws, (cg == 0 && threadIdx.x < 258) ? WS_CK + lane * (WS_C1 - WS_CK) + rg * 128 : OOB, 0, SC1);
        }
        if (w > 4) dma4(lds0 + L_JUNK, w == 7 ? (lane < 18 ? 192 * 1280 + b * 2240 + lane * 128 : OOB) : (lane < 48 ? 192 * 3456 + b * 6048 + lane * 128 : OOB), rtouch, 0);
        if (w > 4) dma4(lds0 + L_JUNK, lane < 32 ? rt_off(lane >> 3, lane & 7) : OOB, us < 54 ? mkrsrc(wd, F_ * D_ * 2) : mkrsrc(wo, D_ * D_ * 2), 0);
        STAMP(12);
        f32x16 acc[4][2] = {};
        f32x2 st[4];
        f32x4 cw[2][4];
        {
            const int xb = (128 * mi + t) * 128 + ((h ^ ((t >> 1) & 7)) << 4);
            const int wb = G1_W + (8 * h + (l15 >> 2)) * 128 + (((4 * ni + 2 * (g4 & 1) + ((lane & 3) >> 1)) ^ ((l15 >> 3) << 2)) << 4) + 8 * (lane & 1);
            u32x4 xf[2][4]; u32x2 wl[2][2], wh[2][2];
            auto rd = [&](int s, int sb, int i) {
                const int e = s & 1;
                const lds_ptr wp = lds + (sb + wb) + s * 2048, xp = lds + (sb + (xb ^ (s << 5)));
                if (i == 0) wl[e][0] = ldstr(wp);
                else if (i == 1) wh[e][0] = ldstr(wp + 512);
                else if (i == 2) xf[e][0] = ldsr128(xp);
                else if (i == 3) wl[e][1] = ldstr(wp + 8192);
                else if (i == 4) wh[e][1] = ldstr(wp + 8192 + 512);
                else xf[e][i - 4] = ldsr128(xp + (i - 4) * 4096);
            };
            auto mm = [&](int s, int i) {
                const int e = s & 1;
                acc[i >> 1][i & 1] = mfma32((u32x4){wl[e][i & 1].x, wl[e][i & 1].y, wh[e][i & 1].x, wh[e][i & 1].y}, xf[e][i >> 1], acc[i >> 1][i & 1]);
                SBAR();
            };
            auto front = [&](int sb) {
#pragma unroll
                for (int s = 0; s < 3; ++s)
#pragma unroll
                    for (int i = 0; i < 8; ++i) {
                        mm(s, i);
                        if (s < 2) rd(s + 1, sb, i);
                        else if (i < 2) { rd(3, sb, 2 * i); rd(3, sb, 2 * i + 1); }
                        else if (i < 6) rd(3, sb, i + 2);
                        SBAR();
                    }
                mm(3, 0); mm(3, 1);
            };
            auto back = [&](bool dord, int nb) {
#pragma unroll
                for (int i = 2; i < 8; ++i) {
                    mm(3, i);
                    if (dord && i < 4) { rd(0, nb, 2 * i - 4); rd(0, nb, 2 * i - 3); }
                    else if (dord) rd(0, nb, i);
                    SBAR();
                }
            };
            asm volatile("s_waitcnt lgkmcnt(0)\n\ts_barrier" ::: "memory");
            STAMP(1);
#pragma unroll
            for (int i = 0; i < 8; ++i) rd(0, 0, i);
            int pb = 2 * G1_SLOT, sb = 0, nb = G1_SLOT;
#pragma unroll 1
            for (int ks = 0; ks < 9; ++ks) {
                SBAR();
                front(sb);
                asm volatile("s_waitcnt vmcnt(0) lgkmcnt(0)\n\ts_barrier" ::: "memory");
                SBAR();
                back(true, nb);
                const int t_ = pb; pb = sb; sb = nb; nb = t_;
            }
            STAMP(14);
#pragma unroll
            for (int tt = 0; tt < 4; ++tt) st[tt] = __builtin_bit_cast(f32x2, __builtin_amdgcn_raw_buffer_load_b64(rws, WS_ST + (256 * rg + 128 * mi + 32 * tt + t) * 8, 0, SC1));
            SBAR();
            front(0);
#pragma unroll
            for (int i = 2; i < 8; ++i) {
                mm(3, i);
                if (i < 6) {
#pragma unroll
                    for (int u = 0; u < 2; ++u) cw[u][i - 2] = ldsf4(lds + L_CS + (u * 128 + 32 * ni + 8 * (i - 2) + 4 * h) * 4) + ldsf4(lds + L_CS + (u * 128 + 64 + 32 * ni + 8 * (i - 2) + 4 * h) * 4);
                }
            }
        }
        STAMP(2);
        if (isq) {
            const unsigned qb = WS_QG + ((8 * rg + 4 * mi) * 80 + 16 * (cg - 27) + 4 * ni) * 512 + lane * 16;
#pragma unroll
            for (int tt = 0; tt < 4; ++tt) {
                const float m = st[tt][0], sc = st[tt][1] * (LOG2E * 0.22360679774997896f);
#pragma unroll
                for (int ua = 0; ua < 4; ++ua) {
                    float y[8];
#pragma unroll
                    for (int j = 0; j < 8; ++j) { const int rr = 8 * (ua & 1) + j; y[j] = (acc[tt][ua >> 1][rr] - m * cw[ua >> 1][rr >> 2][rr & 3]) * sc; }
                    __builtin_amdgcn_raw_buffer_store_b128((u32x4){pk2(y[0], y[1]), pk2(y[2], y[3]), pk2(y[4], y[5]), pk2(y[6], y[7])}, rws,
                                                           qb + tt * 80 * 512 + (ua >> 1) * 4096 + (ua & 1) * 1024, 0, SC1);
                }
            }
        } else {
            const unsigned hb = WS_HF + ((16 * rg + 8 * mi + (t >> 4)) * 54 + 2 * cg + ni) * 1024 + (h * 16 + l15) * 16;
            u32x4 hv[8];
#pragma unroll
            for (int tt = 0; tt < 4; ++tt) {
                const float m = st[tt][0], r = st[tt][1];
                const float rl = -r * LOG2E, ir2 = __builtin_amdgcn_rcpf(r * r);
#pragma unroll
                for (int a = 0; a < 2; ++a) {
                    float y[8];
#pragma unroll
                    for (int j = 0; j < 8; ++j) {
                        const int rr = 8 * a + j;
                        const float zg = acc[tt][0][rr] - m * cw[0][rr >> 2][rr & 3];
                        const float zu = acc[tt][1][rr] - m * cw[1][rr >> 2][rr & 3];
                        const float e = __builtin_amdgcn_exp2f(zg * rl);
                        y[j] = zg * zu * __builtin_amdgcn_rcpf(e * ir2 + ir2);
                    }
                    hv[2 * tt + a] = (u32x4){pk2(y[0], y[1]), pk2(y[2], y[3]), pk2(y[4], y[5]), pk2(y[6], y[7])};
                    PIN(hv[2 * tt + a]);
                }
            }
            __builtin_amdgcn_s_setprio(3);
            lds_wait(lds + L_CNT + 20, 2);
            HDELAY;
#pragma unroll
            for (int j = 0; j < 8; ++j) __builtin_amdgcn_raw_buffer_store_b128(hv[j], rws, hb + (j >> 1) * 2 * 54 * 1024 + (j & 1) * 512, 0, SC1);
        }
        STAMP(3);
        asm volatile("s_waitcnt vmcnt(0)" ::: "memory");
        if (isq) {
            int old = 0;
            if (lane == 0) old = lds_inc(lds + L_CNT + 16);
            if (lane == 0 && old == 3) __builtin_amdgcn_raw_buffer_store_b64(tag, rws, WS_F1 + (rg * 32 + cg) * 128, 0, SC1);
        }
    }
    {
        int old = 0;
        if (lane == 0) old = lds_inc(lds + L_CNT);
        if (__builtin_amdgcn_readfirstlane(old) == 7) arrive(ws, rws, WS_C1 + rg * 128, WS_R1 + rg * 1024, lane, tag);
    }
    STAMP(4);

    const int rt = 4 * xcd + (bi & 3), ct = bi >> 2;
#define LD128(vo, so) __builtin_amdgcn_raw_buffer_load_b128(rws, vo, so, SC1)
    if (w < 4) {
        const int tq = b >> 2, hg = b & 3, hd0 = hg * 8 + 4 * (w >> 1), kh = w & 1;
        const unsigned o16 = WS_KV + kh * 16 * REC + lane * 16, ov = t <= 20 ? WS_KV + kh * 16 * REC + 3072 + h * 336 + t * 16 : OOB;
#define LDK(R, so) do { R.k0 = LD128(o16, so); R.k1 = LD128(o16 + 1024, so); R.kl = LD128(o16 + 2048, so); } while (0)
#define LDV(R, so) do { R.v0 = LD128(ov, so); R.v1 = LD128(ov + 672, so); R.v2 = LD128(ov + 1344, so); R.v3 = LD128(ov + 2016, so); } while (0)
        if (w == 1) {
            const unsigned fo = lane < 8 ? WS_RK + (lane * 8 + xcd) * 128 : WS_F1 + ((tq >> 3) * 32 + 27 + hg + (lane & 1)) * 128;
#pragma unroll 1
            for (int spin = 0; spin < (1 << 18); ++spin) {
                const u32x2 v = __builtin_amdgcn_raw_buffer_load_b64(rws, fo, 0, SC1);
                const u64 ok = __ballot(v.x == tag.x && v.y == tag.y);
                if (ok == ~0ull) break;
                if ((ok & 0xff) == 0xff && lane == 0) *(volatile LAS int*)(lds + L_CNT + 20) = 1;
                __builtin_amdgcn_s_sleep(QSLEEP);
            }
            if (lane == 0) *(volatile LAS int*)(lds + L_CNT + 20) = 2;
            if (b == 0 && lane == 0) __builtin_amdgcn_raw_buffer_store_b64(tag, rws, WS_EP, 0, SC1);
            asm volatile("" ::: "memory");
        } else lds_wait(lds + L_CNT + 20, 1);
        STAMP(13);
        KVf A, B;
        LDK(A, 0); LDV(A, 0); LDK(B, REC);
        if (w == 0) {
            asm volatile("s_mov_b32 m0, %0\n\ts_nop 0\n\tbuffer_load_dword %1, %2, 0 offen sc1 lds" ::"s"(lds0 + L_JUNK), "v"(lane < 45 ? WS_KV + bi * REC + lane * 128 : OOB), "s"(rws) : "memory");
        }
        lds_wait(lds + L_CNT + 20, 2);
        STAMP(8);
        __builtin_amdgcn_s_setprio(APRIO);
        u32x4 qh[4]; u32x2 ql[4];
        {
            u32x4 qr[5];
#pragma unroll
            for (int j = 0; j < 5; ++j) qr[j] = LD128(vl16, WS_QG + (tq * 80 + hd0 / 2 * 5) * 512 + j * 1024);
            auto sw = [&](unsigned lo_from_upper, unsigned up_from_lower) {
                return __builtin_amdgcn_permlane32_swap(lo_from_upper, up_from_lower, false, false);
            };
            qh[0] = qr[0];
            ql[0] = (u32x2){h ? 0u : qr[1].x, h ? 0u : qr[1].y};
            qh[2] = (u32x4){qr[2].z, qr[2].w, qr[3].x, qr[3].y};
            ql[2] = (u32x2){h ? 0u : qr[3].z, h ? 0u : qr[3].w};
            const u32x2 s2 = {qr[1].x, qr[1].y}, s3 = {qr[1].z, qr[1].w}, s4 = {qr[2].x, qr[2].y}, s7 = {qr[3].z, qr[3].w}, s8 = {qr[4].x, qr[4].y}, s9 = {qr[4].z, qr[4].w};
#pragma unroll
            for (int e = 0; e < 2; ++e) {
                const auto a = sw(s2[e], s3[e]), c = sw(s3[e], s4[e]), d = sw(s4[e], 0u);
                qh[1][e] = h ? a[0] : a[1]; qh[1][2 + e] = h ? c[0] : c[1]; ql[1][e] = d[1];
                const auto f = sw(s7[e], s8[e]), g = sw(s8[e], s9[e]), k = sw(s9[e], 0u);
                qh[3][e] = h ? f[0] : f[1]; qh[3][2 + e] = h ? g[0] : g[1]; ql[3][e] = k[1];
            }
        }
        int idx = 0xDDDD; PIN(idx);
        for (int attempt = 0;; ++attempt) {
            B.v3 = (u32x4){0, 0, 0, 0};
            f32x16 O0 = {}, O1 = {}, O2 = {}, O3 = {}, S0, S1, S2, S3 = {};
            S0 = qk_lo<0>(A.kl, ql[0], qk_hi(A.k0, qh[0])); S1 = qk_hi(A.k0, qh[1]);
            PIN(A.k1); PIN(A.v0); PIN(A.v1); PIN(A.v2); PIN(A.v3);
            STAMP(5);
#define RECORD(C, N, VL, KL) do { \
            TILE(S0, S1, S2, S3, 0, C.kl, 1, C.k0, 2, N.v3, O3, C.v0, O0); \
            LDV(N, VL); SBAR(); \
            TILE(S1, S2, S3, S0, 0, C.kl, 2, C.k0, 3, C.v1, O0, C.v0, O1); \
            TILE(S2, S3, S0, S1, 0, C.kl, 3, C.k1, 0, C.v1, O1, C.v0, O2); \
            TILE(S3, S0, S1, S2, 1, C.kl, 0, C.k1, 1, C.v1, O2, C.v0, O3); \
            TILE(S0, S1, S2, S3, 1, C.kl, 1, C.k1, 2, C.v1, O3, C.v2, O0); \
            TILE(S1, S2, S3, S0, 1, C.kl, 2, C.k1, 3, C.v3, O0, C.v2, O1); \
            asm volatile("" : "+v"(N.k0), "+v"(N.k1), "+v"(N.kl)); SBAR(); \
            TILE(S2, S3, S0, S1, 1, C.kl, 3, N.k0, 0, C.v3, O1, C.v2, O2); \
            LDK(C, KL); SBAR(); \
            TILE(S3, S0, S1, S2, 0, N.kl, 0, N.k0, 1, C.v3, O2, C.v2, O3); \
            asm volatile("" : "+v"(N.v0), "+v"(N.v1), "+v"(N.v2), "+v"(N.v3)); SBAR(); } while (0)
            int pk = REC;
#pragma unroll 1
            for (int kb = 0; kb < 8; ++kb) {
                RECORD(A, B, pk, pk + REC);
                RECORD(B, A, pk + REC, pk + 2 * REC);
                pk += 2 * REC;
            }
            O3 = pv<1>(B.v3, S3, O3, idx);
            STAMP(6);
            {
                const lds_ptr op = lds + L_AO + w * 6144 + lane * 16;
                const f32x16& X0 = kh ? O0 : O2; const f32x16& X1 = kh ? O1 : O3;
#pragma unroll
                for (int g = 0; g < 3; ++g) {
                    *(LAS f32x4*)(op + g * 1024) = (f32x4){X0[4 * g], X0[4 * g + 1], X0[4 * g + 2], X0[4 * g + 3]};
                    *(LAS f32x4*)(op + 3072 + g * 1024) = (f32x4){X1[4 * g], X1[4 * g + 1], X1[4 * g + 2], X1[4 * g + 3]};
                }
            }
            asm volatile("s_waitcnt lgkmcnt(0)\n\ts_barrier" ::: "memory");
            bool bad = false;
            u32x2 oo[2][3];
#pragma unroll
            for (int i = 0; i < 2; ++i) {
                const f32x16& Y = kh ? (i ? O3 : O2) : (i ? O1 : O0);
                const lds_ptr ip = lds + L_AO + (w ^ 1) * 6144 + i * 3072 + lane * 16;
                f32x4 z[3];
#pragma unroll
                for (int g = 0; g < 3; ++g) z[g] = ldsf4(ip + g * 1024) + (f32x4){Y[4 * g], Y[4 * g + 1], Y[4 * g + 2], Y[4 * g + 3]};
                const auto sw_ = __builtin_amdgcn_permlane32_swap(__float_as_uint(z[2][0]), __float_as_uint(z[2][0]), false, false);
                const float l = __uint_as_float(sw_[1]);
                const float il = __builtin_amdgcn_rcpf(l);
                float chk = 0.f;
#pragma unroll
                for (int j = 0; j < 4; ++j) chk = __builtin_fmaf(z[0][j], 0.f, __builtin_fmaf(z[1][j], 0.f, __builtin_fmaf(z[2][j], 0.f, chk)));
                bad |= !(l >= 9.5367431640625e-07f && l <= 1.2676506002282294e30f && chk == 0.f);
#pragma unroll
                for (int g = 0; g < 3; ++g) oo[i][g] = (u32x2){pk2(z[g][0] * il, z[g][1] * il), pk2(z[g][2] * il, z[g][3] * il)};
            }
            {
                const unsigned ob = WS_OF + (2 * tq + (t >> 4)) * 20480 + (t & 15) * 16 + (5 * (hg * 4 + w) + h) * 256;
                __builtin_amdgcn_raw_buffer_store_b128((u32x4){oo[0][0].x, oo[0][0].y, oo[0][1].x, oo[0][1].y}, rws, ob, 0, SC1);
                __builtin_amdgcn_raw_buffer_store_b128((u32x4){oo[1][0].x, oo[1][0].y, oo[1][1].x, oo[1][1].y}, rws, ob + 512, 0, SC1);
                __builtin_amdgcn_raw_buffer_store_b128((u32x4){oo[0][2].x, oo[0][2].y, oo[1][2].x, oo[1][2].y}, rws, h ? OOB : ob + 1024, 0, SC1);
            }
            if (attempt == 0 && __any(bad) && lane == 0) *(LAS int*)(lds + L_CNT + 4) = 1;
            asm volatile("s_waitcnt vmcnt(0) lgkmcnt(0)\n\ts_barrier" ::: "memory");
            if (attempt == 1 || __builtin_amdgcn_readfirstlane(*(volatile LAS int*)(lds + L_CNT + 4)) == 0) break;
            float mxs[4];
#pragma unroll
            for (int i = 0; i < 4; ++i) mxs[i] = -__builtin_inff();
#pragma unroll 1
            for (int kb = 0; kb < 32; ++kb) {
                const unsigned ko = WS_KV + lane * 16;
                const u32x4 k0 = LD128(ko, kb * REC), k1 = LD128(ko + 1024, kb * REC), kl = LD128(ko + 2048, kb * REC);
#pragma unroll
                for (int i = 0; i < 4; ++i) {
                    const f32x16 s0 = qk_lo<0>(kl, ql[i], qk_hi(k0, qh[i])), s1 = qk_lo<1>(kl, ql[i], qk_hi(k1, qh[i]));
#pragma unroll
                    for (int r = 0; r < 16; ++r) mxs[i] = fmaxf(mxs[i], fmaxf(s0[r], s1[r]));
                }
            }
#pragma unroll
            for (int i = 0; i < 4; ++i) {
                const float m = -fmaxf(mxs[i], __shfl_xor(mxs[i], 32));
                const float a0 = bfr(m), a1 = bfr(m - a0), a2 = bfr(m - a0 - a1);
                if (h) ql[i] = (u32x2){pk2(a0, a1), pk2(a2, 0.f)};
            }
            LDK(A, 0); LDV(A, 0); LDK(B, REC);
        }
        if (threadIdx.x == 0) __builtin_amdgcn_raw_buffer_store_b64(tag, rws, WS_F2 + b * 128, 0, SC1);
        STAMP(7);
        const int row = 4 * w + min(lane / 10, 3), cc = lane % 10;
        const int la = row * 336 + cc * 32;
        const unsigned go = lane < 40 ? (64 * rt + row) * 1280 + 160 * ct + cc * 16 : OOB;
        u32x4 xv[4]; f32x4 gate[4][2];
#pragma unroll
        for (int k = 0; k < 4; ++k) xv[k] = __builtin_amdgcn_raw_buffer_load_b128(rx, go, ((k + ct) & 3) * 16 * 1280, 0);
        auto sum4 = [&](int k, int e) {
            const lds_ptr p = lds + ((k + ct) & 3) * PT + la + e * 16;
            return (ldsf4(p) + ldsf4(p + GR_W)) + (ldsf4(p + 2 * GR_W) + ldsf4(p + 3 * GR_W));
        };
#pragma unroll
        for (int j = 0; j < 8; ++j) gate[j >> 1][j & 1] = sum4(j >> 1, j & 1);
        asm volatile("s_waitcnt lgkmcnt(0)\n\ts_barrier" : "+v"(gate[0][0]), "+v"(gate[0][1]), "+v"(gate[1][0]), "+v"(gate[1][1]), "+v"(gate[2][0]), "+v"(gate[2][1]), "+v"(gate[3][0]), "+v"(gate[3][1])::"memory");
#pragma unroll
        for (int j = 0; j < 8; ++j) {
#pragma unroll
            for (int r = 0; r < 4; ++r) gate[j >> 1][j & 1][r] *= __builtin_amdgcn_rcpf(1.f + __builtin_amdgcn_exp2f(-gate[j >> 1][j & 1][r] * LOG2E));
            PIN(gate[j >> 1][j & 1]);
        }
        asm volatile("" : "+v"(xv[0]), "+v"(xv[1]), "+v"(xv[2]), "+v"(xv[3]));
        STAMP(9);
        const rsrc_t rout = mkrsrc(out, S_ * D_ * 2);
#pragma unroll
        for (int k = 0; k < 4; ++k) {
            asm volatile("s_barrier" ::: "memory");
            if (k == 3) STAMP(11);
            u32x4 o;
#pragma unroll
            for (int e = 0; e < 2; ++e) {
                const f32x4 s = sum4(k, e), gt = gate[k][e];
                o[2 * e] = pk2(bflo(xv[k][2 * e]) + s[0] * gt[0], bfhi(xv[k][2 * e]) + s[1] * gt[1]);
                o[2 * e + 1] = pk2(bflo(xv[k][2 * e + 1]) + s[2] * gt[2], bfhi(xv[k][2 * e + 1]) + s[3] * gt[3]);
            }
            __builtin_amdgcn_raw_buffer_store_b128(o, rout, go + ((k + ct) & 3) * 16 * 1280, 0, 0);
        }
        STAMP(15);
    } else {
        const int gw = w - 4;
        const int ks0 = gw * 13 + min(gw, 2), n = gw < 2 ? 14 : 13;
        if (w == 4) {
            wait_flags<GSLEEP>(rws, WS_R1 + ((lane & 7) * 8 + xcd) * 128, true, tag);
            if (lane == 0) *(volatile LAS int*)(lds + L_CNT + 24) = 1;
        } else lds_wait(lds + L_CNT + 24, 1);
        STAMP(5);
        __builtin_amdgcn_s_setprio(GPRIO);
        f32x4 dacc[5][4] = {};
        {
            u32x4 hA[4], hB[4], wA[5], wB[5];
            const unsigned hb = WS_HF + (4 * rt * 54 + ks0) * 1024, wb = WS_WF + (ks0 * 40 + 5 * ct) * 1024;
#pragma unroll
            for (int j = 0; j < 4; ++j) hA[j] = LD128(vl16, hb + j * 54 * 1024);
#pragma unroll
            for (int j = 0; j < 5; ++j) wA[j] = LD128(vl16, wb + j * 1024);
#pragma unroll
            for (int j = 0; j < 4; ++j) hB[j] = LD128(vl16, hb + (j * 54 + 1) * 1024);
#pragma unroll
            for (int j = 0; j < 5; ++j) wB[j] = LD128(vl16, wb + (40 + j) * 1024);
#define DSTEP(W, H, i, LOAD) do { \
                asm volatile("" : "+v"(H[0]), "+v"(H[1]), "+v"(H[2]), "+v"(H[3]), "+v"(W[0]), "+v"(W[1]), "+v"(W[2]), "+v"(W[3]), "+v"(W[4])); \
                const int nx = min((i) + 2, n - 1); \
                _Pragma("unroll") for (int k = 0; k < 20; ++k) { \
                    dacc[k % 5][k / 5] = mfma16(W[k % 5], H[k / 5], dacc[k % 5][k / 5]); \
                    SBAR(); \
                    if (LOAD && k % 5 == 4) H[k / 5] = LD128(vl16, hb + ((k / 5) * 54 + nx) * 1024); \
                    if (LOAD && k >= 15) W[k - 15] = LD128(vl16, wb + (nx * 40 + k - 15) * 1024); \
                    GPACE; \
                    SBAR(); \
                } } while (0)
#pragma unroll 1
            for (int i = 0; i < 12; i += 2) { DSTEP(wA, hA, i, true); DSTEP(wB, hB, i + 1, true); }
            DSTEP(wA, hA, 12, false);
            if (n == 14) DSTEP(wB, hB, 13, false);
        }
        STAMP(6);
        __builtin_amdgcn_s_setprio(3);
#pragma unroll
        for (int nt = 0; nt < 5; ++nt)
#pragma unroll
            for (int tt = 0; tt < 4; ++tt) *(LAS f32x4*)(lds + gw * GR_W + tt * PT + l15 * 336 + g4 * 16 + nt * 64) = dacc[nt][tt];
        u32x4 wf[25];
#pragma unroll
        for (int j = 0; j < 25; ++j) wf[j] = LD128(vl16, WS_WF + ((54 + gw * 5 + j / 5) * 40 + 5 * ct + j % 5) * 1024);
        STAMP(7);
        asm volatile("s_waitcnt lgkmcnt(0)\n\ts_barrier" ::: "memory");
        asm volatile("s_barrier" ::: "memory");
        if (__builtin_amdgcn_readfirstlane(*(volatile LAS int*)(lds + L_CNT + 4)) != 0) {
            asm volatile("s_barrier" ::: "memory");
            asm volatile("s_barrier" ::: "memory");
        }
        asm volatile("s_barrier" ::: "memory");
        STAMP(9);
        wait_flags<1>(rws, WS_F2 + (8 * rt + 4 * (lane & 1) + gw) * 128, true, tag);
        STAMP(10);
        u32x4 of[4][5];
        auto ldo = [&](int k, int s) { of[k][s] = LD128(vl16, WS_OF + ((4 * rt + ((k + ct) & 3)) * 20 + gw * 5 + s) * 1024); };
#pragma unroll
        for (int s = 0; s < 10; ++s) ldo(s / 5, s % 5);
        f32x4 sacc[2][5];
#pragma unroll
        for (int k = 0; k < 5; ++k) {
#pragma unroll
            for (int s = 0; s < 5; ++s) {
                SBAR();
                if (k < 2) ldo(k + 2, s);
                if (k > 0) *(LAS f32x4*)(lds + gw * GR_W + ((k - 1 + ct) & 3) * PT + l15 * 336 + g4 * 16 + s * 64) = sacc[(k - 1) & 1][s];
                SBAR();
                if (k < 4) {
#pragma unroll
                    for (int nt = 0; nt < 5; ++nt) sacc[k & 1][nt] = mfma16(wf[s * 5 + nt], of[k][s], s == 0 ? (f32x4){} : sacc[k & 1][nt]);
                }
            }
            SBAR();
            if (k == 4) STAMP(11);
            if (k > 0) asm volatile("s_waitcnt lgkmcnt(0)\n\ts_barrier" ::: "memory");
        }
        STAMP(15);
    }
    {
        FLUSH(0); FLUSH(1); FLUSH(2); FLUSH(3); FLUSH(4); FLUSH(5); FLUSH(6); FLUSH(7); FLUSH(8); FLUSH(9); FLUSH(10); FLUSH(11); FLUSH(12); FLUSH(13); FLUSH(14); FLUSH(15);
    }
}

extern "C" void kernel_launch(void* const* d_in, const int* in_sizes, int n_in, void* d_out, int out_size, void* d_ws, size_t ws_size, hipStream_t stream) {
    (void)hipFuncSetAttribute((const void*)fused_block, hipFuncAttributeMaxDynamicSharedMemorySize, LDS_BYTES);
    fused_block<<<256, 512, LDS_BYTES, stream>>>((const u16*)d_in[0], (const u16*)d_in[1], (const u16*)d_in[2], (const u16*)d_in[3], (const u16*)d_in[4],
                                                 (const u16*)d_in[6], (const u16*)d_in[7], (const u16*)d_in[8], (char*)d_ws, (u16*)d_out);
}
```
